```python
import torch

def _softmax(x, axis=-1):
    return torch.softmax(x, dim=axis)
def _gelu(x):
    return torch.nn.functional.gelu(x, approximate="tanh")
def _silu(x):
    return torch.nn.functional.silu(x)
def _sigmoid(x):
    return torch.sigmoid(x)
def _layernorm(x, axis=-1, eps=1e-5):
    m = x.mean(dim=axis, keepdim=True)
    v = x.var(dim=axis, keepdim=True, unbiased=False)
    return (x - m) / torch.sqrt(v + eps)
def _scatter_add(tgt, idx, src, axis=0):
    return tgt.clone().index_add_(axis, idx, src)
def _segment_sum(src, seg, n):
    out = torch.zeros((n,) + src.shape[1:], dtype=src.dtype)
    return out.index_add_(0, seg, src)
def _pad(x, pw):
    flat = tuple(v for pair in reversed(pw) for v in pair)
    return torch.nn.functional.pad(x, flat)

kernel_name = 'standalone__molecular_angular_gemnet__graph'

def setup_inputs():

    _w = (lambda a: (lambda t: t.to(torch.bfloat16) if t.is_floating_point() else t)(torch.as_tensor(a)))
    N, deg, De, L = 10000, 32, 16, 1
    E = N * deg
    T = E * deg
    NR = 8
    dist = torch.rand(E).to(float)
    angle = (torch.rand(T) * torch.pi).to(float)
    centers = torch.linspace(0.0, 1.0, NR).to(float)
    ang_freq = torch.arange(1, L + 1).to(float)
    return {
        "m": _w((lambda *s: torch.randn(*s, dtype=torch.bfloat16) * 0.05)(E, De)),
        "idx_in": _w((lambda lo, hi, *s: torch.randint(lo, hi, s))(0, E, T).to(torch.int64)),
        "idx_out": _w((lambda lo, hi, *s: torch.randint(lo, hi, s))(0, E, T).to(torch.int64)),
        "dist": _w(dist),
        "angle": _w(angle),
        "centers": _w(centers),
        "ang_freq": _w(ang_freq),
        "w_rbf": _w((lambda *s: torch.randn(*s, dtype=torch.bfloat16) * 0.2)(NR, De)),
        "w_sbf": _w((lambda *s: torch.randn(*s, dtype=torch.bfloat16) * 0.2)(NR * L, De)),
        "w_down": _w((lambda *s: torch.randn(*s, dtype=torch.bfloat16) * 0.2)(De, De)),
        "w_up": _w((lambda *s: torch.randn(*s, dtype=torch.bfloat16) * 0.2)(De, De)),
    }

def reference(m, idx_in, idx_out, dist, angle, centers, ang_freq, w_rbf, w_sbf, w_down, w_up):
    E, De = m.shape[0], m.shape[1]
    NR = centers.shape[0]
    L = ang_freq.shape[0]
    rbf = torch.exp(-10.0 * (dist[:, None] - centers[None, :]) ** 2)
    cbf = torch.cos(ang_freq[None, :] * angle[:, None])
    rbf_t = rbf[idx_out]
    sbf = torch.reshape(rbf_t[:, :, None] * cbf[:, None, :], (-1, NR * L))
    x = (m[idx_in] @ w_down) * (sbf @ w_sbf)
    agg = _segment_sum(x, idx_out, E)
    return m + (agg @ w_up) * (rbf @ w_rbf)
```

```cpp
#include <hip/hip_runtime.h>
#include <hip/hip_bf16.h>
#include <stdint.h>

__device__ __forceinline__ float bf2f(uint16_t v) { return __uint_as_float(((uint32_t)v) << 16); }

__global__ void k_scatter(const uint16_t* __restrict__ m, const int* __restrict__ idx_in, const int* __restrict__ idx_out,
                          const uint16_t* __restrict__ angle, const uint16_t* __restrict__ ang_freq,
                          float* __restrict__ acc, int T) {
    int g = (blockIdx.x * blockDim.x + threadIdx.x);
    int t = g >> 4, k = g & 15;
    if (t >= T) return;
    float f = bf2f(ang_freq[0]);
    float c = cosf(f * bf2f(angle[t]));
    int i = idx_in[t], o = idx_out[t];
    atomicAdd(&acc[(size_t)o * 16 + k], c * bf2f(m[(size_t)i * 16 + k]));
}

__global__ __launch_bounds__(256) void k_epi(const uint16_t* __restrict__ m, const uint16_t* __restrict__ dist, const uint16_t* __restrict__ centers,
                      const uint16_t* __restrict__ w_rbf, const uint16_t* __restrict__ w_sbf,
                      const uint16_t* __restrict__ w_down, const uint16_t* __restrict__ w_up,
                      const float* __restrict__ acc, __hip_bfloat16* __restrict__ out, int E) {
    __shared__ float s_agg[256];
    int g = blockIdx.x * blockDim.x + threadIdx.x;
    int e = g >> 4, j = g & 15;
    int ee = e < E ? e : E - 1;
    float d = bf2f(dist[ee]);
    float md = 0.f, s = 0.f, gt = 0.f;
    for (int k = 0; k < 16; ++k) md += acc[(size_t)ee * 16 + k] * bf2f(w_down[k * 16 + j]);
    for (int r = 0; r < 8; ++r) { float x = d - bf2f(centers[r]); float rb = expf(-10.f * x * x);
        s += rb * bf2f(w_sbf[r * 16 + j]); gt += rb * bf2f(w_rbf[r * 16 + j]); }
    s_agg[threadIdx.x] = md * s;
    __syncthreads();
    float u = 0.f;
    for (int k = 0; k < 16; ++k) u += s_agg[(threadIdx.x & ~15) + k] * bf2f(w_up[k * 16 + j]);
    if (e < E) out[(size_t)e * 16 + j] = __float2bfloat16(bf2f(m[(size_t)e * 16 + j]) + u * gt);
}

extern "C" void kernel_launch(void* const* d_in, const int* in_sizes, int n_in,
                              void* d_out, int out_size, void* d_ws, size_t ws_size,
                              hipStream_t stream) {
    const uint16_t* m = (const uint16_t*)d_in[0];
    const int* idx_in = (const int*)d_in[1];
    const int* idx_out = (const int*)d_in[2];
    const uint16_t* dist = (const uint16_t*)d_in[3];
    const uint16_t* angle = (const uint16_t*)d_in[4];
    const uint16_t* centers = (const uint16_t*)d_in[5];
    const uint16_t* ang_freq = (const uint16_t*)d_in[6];
    const uint16_t* w_rbf = (const uint16_t*)d_in[7];
    const uint16_t* w_sbf = (const uint16_t*)d_in[8];
    const uint16_t* w_down = (const uint16_t*)d_in[9];
    const uint16_t* w_up = (const uint16_t*)d_in[10];
    int E = in_sizes[3];
    int T = in_sizes[1];
    float* acc = (float*)d_ws;
    hipMemsetAsync(acc, 0, (size_t)E * 16 * 4, stream);
    long long n = (long long)T * 16;
    k_scatter<<<(int)((n + 255) / 256), 256, 0, stream>>>(m, idx_in, idx_out, angle, ang_freq, acc, T);
    k_epi<<<(E * 16 + 255) / 256, 256, 0, stream>>>(m, dist, centers, w_rbf, w_sbf, w_down, w_up, acc, (__hip_bfloat16*)d_out, E);
}
```

```cpp
#include <hip/hip_runtime.h>
#include <hip/hip_bf16.h>
#include <stdint.h>

typedef unsigned int u32;
typedef unsigned long long u64;
typedef unsigned short u16;
using u32x2 = __attribute__((ext_vector_type(2))) unsigned;
using u32x3 = __attribute__((ext_vector_type(3))) unsigned;
using u32x4 = __attribute__((ext_vector_type(4))) unsigned;
using f32x4 = __attribute__((ext_vector_type(4))) float;
using bf16x8 = __attribute__((ext_vector_type(8))) __bf16;
using s16x4 = __attribute__((ext_vector_type(4))) short;
typedef __attribute__((address_space(3))) u64 lds_u64;
typedef __attribute__((address_space(3))) u32 lds_u32;
typedef __attribute__((address_space(3))) u16 lds_u16;
typedef __attribute__((address_space(3))) double lds_f64;
typedef __attribute__((address_space(3))) u32x2 lds_u32x2;
typedef __attribute__((address_space(3))) u32x4 lds_u32x4;

namespace {

constexpr int E_ = 320000;
constexpr int T_ = 10240000;
constexpr int NB = 256;
constexpr int RB = 1250;
#define NSLICE 5
constexpr int S = NSLICE;
constexpr int SR = S == 5 ? 65536 : (E_ + S - 1) / S;
constexpr u32 SBYTES = (u32)SR * 32u;
constexpr int MAGIC_SK = S == 5 ? 0 : (SR >= 32768 ? 7 : 6);
constexpr u32 MAGIC_SR = S == 5 ? 0u : (u32)((((u64)1 << (32 + MAGIC_SK)) + SR - 1) / SR);
static_assert(S <= 8 && SR <= 65536 && MAGIC_SR < (1u << 24), "slices");
constexpr int CH = 20480;
constexpr int CHP2 = (CH + NB * S) / 2;
constexpr int NC = 500;
constexpr int NWG1 = 250;
constexpr u32 MAGIC_RB = 3435974u;

constexpr size_t WS_REC = 0;
constexpr size_t WS_TAB = (size_t)NC * CHP2 * 12 + 4096;
constexpr size_t WS_META = WS_TAB + (size_t)NC * NB * S * 4;
static_assert(WS_TAB % 128 == 0 && WS_META % 128 == 0, "alignment");

constexpr u32 L1_LO = 0;
constexpr u32 L1_C = CHP2 * 8;
constexpr u32 L1_H = CHP2 * 12;
constexpr u32 L1_TAB = L1_H + 16384;
constexpr u32 L1_W = L1_TAB + NB * S * 4;
constexpr u32 L1_SIZE = L1_W + 128;

#define NSB_ (NSLICE == 5 ? 5 : (NSLICE == 6 ? 4 : 3))
constexpr int NSB = NSB_;
constexpr int CAPW = NSB * 64;
constexpr u32 L2_ACC = 0;
constexpr u32 L2_CNT = (RB + 16) * 64;
constexpr u32 L2_MARK = L2_CNT + 5072;
constexpr u32 L2_FLAG = L2_MARK + 16 * (CAPW + 64) * 4;
constexpr u32 L2_PROG = L2_FLAG + 16;
constexpr u32 L2_NW = L2_FLAG + 64;
constexpr u32 L2_SIZE = L2_FLAG + 64 + 8 * 64;
static_assert(L2_SIZE <= 163840 && L1_SIZE <= 163840 && L2_SIZE % 16 == 0, "LDS");

#define REC_AUX 0
#define NO_PACE
#define ITEM_BARRIER_B
constexpr float KMAGIC = 12582912.0f;
constexpr u32 KBITS = 0x4B400000u;

__device__ __forceinline__ lds_u32* L32(u32 a) { return (lds_u32*)(size_t)a; }
__device__ __forceinline__ lds_u64* L64(u32 a) { return (lds_u64*)(size_t)a; }
__device__ __forceinline__ lds_u16* L16(u32 a) { return (lds_u16*)(size_t)a; }
__device__ __forceinline__ void lds_add32(u32 a, u32 v) { __hip_atomic_fetch_add(L32(a), v, __ATOMIC_RELAXED, __HIP_MEMORY_SCOPE_WORKGROUP); }
__device__ __forceinline__ u32 lds_add32_rtn(u32 a, u32 v) { return __hip_atomic_fetch_add(L32(a), v, __ATOMIC_RELAXED, __HIP_MEMORY_SCOPE_WORKGROUP); }
__device__ __forceinline__ void lds_add64(u32 a, u64 v) { __hip_atomic_fetch_add(L64(a), v, __ATOMIC_RELAXED, __HIP_MEMORY_SCOPE_WORKGROUP); }
__device__ __forceinline__ float bf2f(u32 v) { return __uint_as_float(v << 16); }

template <int CTRL, int RM = 0xf, bool BC = true>
__device__ __forceinline__ u32 dpp0(u32 v) { return (u32)__builtin_amdgcn_update_dpp(0, (int)v, CTRL, RM, 0xf, BC); }
template <int J>
__device__ __forceinline__ u32 quad_bcast(u32 v) { return (u32)__builtin_amdgcn_mov_dpp((int)v, J * 0x55, 0xf, 0xf, true); }

__device__ __forceinline__ u32 wave_scan_add(u32 v) {
    v += dpp0<0x111>(v); v += dpp0<0x112>(v); v += dpp0<0x114>(v); v += dpp0<0x118>(v);
    v += dpp0<0x142, 0xa, false>(v); v += dpp0<0x143, 0xc, false>(v);
    return v;
}
__device__ __forceinline__ u32 wave_scan_max(u32 v) {
    v = max(v, dpp0<0x111>(v)); v = max(v, dpp0<0x112>(v)); v = max(v, dpp0<0x114>(v)); v = max(v, dpp0<0x118>(v));
    v = max(v, dpp0<0x142, 0xa, false>(v)); v = max(v, dpp0<0x143, 0xc, false>(v));
    return v;
}
using f32x2 = __attribute__((ext_vector_type(2))) float;
using bf16x2 = __attribute__((ext_vector_type(2))) __bf16;
__device__ __forceinline__ u32 cvt_pk_bf16(float lo, float hi) {
    return __builtin_bit_cast(u32, __builtin_convertvector((f32x2){lo, hi}, bf16x2));
}
__device__ __forceinline__ float dot2_bf16(u32 a, u32 b, float c) {
    float r; asm("v_dot2_f32_bf16 %0, %1, %2, %3" : "=v"(r) : "v"(a), "v"(b), "s"(c)); return r;
}

__device__ __forceinline__ float cos_rev(float x) {
    const float C_HI = 0.15915493667125702f;
    const float C_LO = 6.4206382985757765e-09f;
    const float n = __builtin_rintf(x * C_HI);
    float r = __builtin_fmaf(x, C_HI, -n);
    r = __builtin_fmaf(x, C_LO, r);
    return __builtin_amdgcn_cosf(r);
}

#define ST_AUX 0
#define ST_OUT(p, v) (*(p) = (v))
#define STAMP(i)
struct Raw { u32x4 o[5]; u32x4 i[5]; u32x2 a[5]; };

struct P1In { __amdgpu_buffer_rsrc_t io, ii, an; };
__device__ __forceinline__ void p1_load_group(Raw& r, int u, const P1In& in, int chunk, int tid) {
    const u32 q = chunk * (CH / 4) + u * 1024 + tid;
    r.o[u] = __builtin_amdgcn_raw_buffer_load_b128(in.io, q * 16, 0, 2);
    r.i[u] = __builtin_amdgcn_raw_buffer_load_b128(in.ii, q * 16, 0, 2);
    r.a[u] = __builtin_amdgcn_raw_buffer_load_b64(in.an, q * 8, 0, 2);
    __builtin_amdgcn_sched_barrier(0);
}

template <bool FIRST>
__device__ __forceinline__ void p1_chunk(Raw& raw, int chunk, int next_chunk, const P1In& in, float freq,
                                         float& nanacc, const u32x4* __restrict__ m4, char* __restrict__ rec,
                                         u32* __restrict__ tab, int tid
                                         ) {
    constexpr u32 H = L1_H + (FIRST ? 0 : 8192);
    const int lane = tid & 63, wave = tid >> 6;
    u32 lo[20], hi[20];
#pragma unroll
    for (int u = 0; u < 5; ++u) {
#pragma unroll
        for (int j = 0; j < 4; ++j) {
            const u32 o = raw.o[u][j], i = raw.i[u][j];
            const u32 aw = raw.a[u][j >> 1];
            const float a = (j & 1) ? __uint_as_float(aw & 0xffff0000u) : __uint_as_float(aw << 16);
            const float c = cos_rev(a * freq);
            nanacc = __builtin_fmaf(c, 0.0f, nanacc);
            const u32 b = (u32)(((u64)(o & 0xffffffu) * MAGIC_RB) >> 32);
            u32 ol;
            asm("v_mad_i32_i24 %0, %1, %2, %3" : "=v"(ol) : "v"(b), "s"(-RB), "v"(o));
            const u32 cb = cvt_pk_bf16(c, 0.0f);
            if constexpr (S == 5) {
                lo[u * 4 + j] = (i & 0xffffu) | (ol << 16);
                hi[u * 4 + j] = ((i << 10) & 0xfc000000u) | (b << 18) | cb;
            } else {
                const u32 sl = (u32)(((u64)(i & 0xffffffu) * MAGIC_SR) >> 32) >> MAGIC_SK;
                u32 il;
                asm("v_mad_i32_i24 %0, %1, %2, %3" : "=v"(il) : "v"(sl), "s"(-SR), "v"(i));
                lo[u * 4 + j] = il | (ol << 16);
                hi[u * 4 + j] = (sl << 26) | (b << 18) | cb;
            }
            lds_add32(H + (hi[u * 4 + j] >> 16), 1u);
        }
#pragma unroll
        for (int j = 0; j < 4; ++j) asm volatile("" : "+v"(hi[u * 4 + j]), "+v"(lo[u * 4 + j]));
        __builtin_amdgcn_sched_barrier(0);
        if (FIRST) p1_load_group(raw, u, in, next_chunk, tid);
    }
    u32x4 mv[3];
    if (!FIRST) {
        constexpr int PER = E_ * 2 / NWG1;
        mv[0] = m4[blockIdx.x * PER + tid];
        mv[1] = m4[blockIdx.x * PER + 1024 + tid];
        mv[2] = m4[blockIdx.x * PER + min(2048 + tid, PER - 1)];
        __builtin_amdgcn_sched_barrier(0);
    }
    STAMP(0);
    __syncthreads();
    STAMP(1);
    const u32x2 cnt = *(lds_u32x2*)(size_t)(H + tid * 8);
    const u32 p0 = (cnt[0] + 1) & ~1u, p1 = (cnt[1] + 1) & ~1u;
    const u32 sum = p0 + p1;
    const u32 incl = wave_scan_add(sum);
    if (lane == 63) *L32(L1_W + wave * 4) = incl;
    __syncthreads();
    const u32 wraw = *L32(L1_W + (lane & 15) * 4);
    const u32 wsum = wave_scan_add(lane < 16 ? wraw : 0u);
    const u32 total = __builtin_amdgcn_readlane(wsum, 15);
    const u32 woff = __builtin_amdgcn_readlane(wsum - wraw, __builtin_amdgcn_readfirstlane(wave));
    const u32 st0 = woff + incl - sum, st1 = st0 + p0;
    *(lds_u32x2*)(size_t)(H + tid * 8) = (u32x2){st0, st1};
    if (tid < NB * S / 2) {
        const int s = (2 * tid) >> 8, b = (2 * tid) & 255;
        *L32(L1_TAB + (b * S + s) * 4) = (st0 >> 1) | (p0 << 15);
        *L32(L1_TAB + ((b + 1) * S + s) * 4) = (st1 >> 1) | (p1 << 15);
        if (cnt[0] & 1) { *L32(L1_LO + (st0 + cnt[0]) * 4) = (u32)RB << 16; *L16(L1_C + (st0 + cnt[0]) * 2) = 0; }
        if (cnt[1] & 1) { *L32(L1_LO + (st1 + cnt[1]) * 4) = (u32)RB << 16; *L16(L1_C + (st1 + cnt[1]) * 2) = 0; }
    }
    __syncthreads();
    STAMP(2);
    if (tid < NB * S / 4) ST_OUT((u32x4*)(tab + (size_t)chunk * NB * S) + tid, *(lds_u32x4*)(size_t)(L1_TAB + tid * 16));
#pragma unroll
    for (int r0 = 0; r0 < 20; r0 += 5) {
        u32 pos[5];
#pragma unroll
        for (int r = 0; r < 5; ++r) pos[r] = lds_add32_rtn(H + (hi[r0 + r] >> 16), 1u);
#pragma unroll
        for (int r = 0; r < 5; ++r) {
            *L32(L1_LO + pos[r] * 4) = lo[r0 + r];
            *L16(L1_C + pos[r] * 2) = (u16)hi[r0 + r];
        }
        __builtin_amdgcn_sched_barrier(0);
    }
    if (!FIRST) {
        u32 mx = 0;
#pragma unroll
        for (int u = 0; u < 3; ++u)
#pragma unroll
            for (int k = 0; k < 4; ++k) mx = max(mx, max(mv[u][k] & 0x7fffu, (mv[u][k] >> 16) & 0x7fffu));
        mx = wave_scan_max(mx);
        if (lane == 63) __hip_atomic_fetch_max(L32(L1_W + 64), mx, __ATOMIC_RELAXED, __HIP_MEMORY_SCOPE_WORKGROUP);
        if (nanacc != nanacc) *L32(L1_W + 68) = 1u;
    }
    STAMP(3);
    __syncthreads();
    STAMP(4);
    const auto rso = __builtin_amdgcn_make_buffer_rsrc((void*)(rec + (size_t)chunk * (CHP2 * 12)), 0, (u32)CHP2 * 12u, 0x00020000);
    const int npair = total >> 1;
#pragma unroll
    for (int u = 0; u < 11; ++u) {
        const int j = u * 1024 + tid;
        if (u < 10 || j < npair) {
            const u32x2 l = *(lds_u32x2*)(size_t)(L1_LO + j * 8);
            const u32 cc = *L32(L1_C + j * 4);
            const u32x3 d = {l[0], __builtin_amdgcn_perm(l[1], cc, 0x05040100u), __builtin_amdgcn_perm(cc, l[1], 0x07060302u)};
            __builtin_amdgcn_raw_buffer_store_b96(d, rso, j * 12, 0, ST_AUX);
        }
    }
    STAMP(5);
}

__global__ __launch_bounds__(1024) void p1_partition(const u32x4* __restrict__ ii, const u32x4* __restrict__ io,
                                                     const u32x2* __restrict__ an, const u16* __restrict__ ang_freq,
                                                     const u32x4* __restrict__ m4, char* __restrict__ rec,
                                                     u32* __restrict__ tab, u32* __restrict__ meta) {
    const int tid = threadIdx.x;
    const int c0 = blockIdx.x, c1 = blockIdx.x + NWG1;
    const u32 fbits = ang_freq[0];
    __builtin_amdgcn_sched_barrier(0);
    P1In in;
    in.io = __builtin_amdgcn_make_buffer_rsrc((void*)io, 0, (u32)T_ * 4u, 0x00020000);
    in.ii = __builtin_amdgcn_make_buffer_rsrc((void*)ii, 0, (u32)T_ * 4u, 0x00020000);
    in.an = __builtin_amdgcn_make_buffer_rsrc((void*)an, 0, (u32)T_ * 2u, 0x00020000);
    Raw raw;
#pragma unroll
    for (int u = 0; u < 5; ++u) p1_load_group(raw, u, in, c0, tid);
    *(lds_u32x4*)(size_t)(L1_H + tid * 16) = (u32x4){0u, 0u, 0u, 0u};
    if (tid < 2) *L32(L1_W + 64 + tid * 4) = 0u;
    const float freq = bf2f(fbits);
    __syncthreads();
    float nanacc = 0.0f;
    p1_chunk<true>(raw, c0, c1, in, freq, nanacc, m4, rec, tab, tid);
    p1_chunk<false>(raw, c1, c1, in, freq, nanacc, m4, rec, tab, tid);
    if (tid == 0) meta[blockIdx.x] = *L32(L1_W + 64) | (*L32(L1_W + 68) << 16);
    if (blockIdx.x == 0 && tid >= 64 && tid < 64 + 256 - NWG1) meta[NWG1 + tid - 64] = 0u;
}

struct EpiW { bf16x8 wd, wu; s16x4 ws, wr; u32 cen; };

__device__ __forceinline__ u32 pack_bf(u32 a, u32 b) { return a | (b << 16); }

__device__ __forceinline__ void epi_weights(EpiW& w, const u16* __restrict__ w_rbf, const u16* __restrict__ w_sbf,
                                            const u16* __restrict__ w_down, const u16* __restrict__ w_up,
                                            const u16* __restrict__ centers, int lane) {
    const int fo = lane & 15, g = lane >> 4;
    u32x4 d, u;
    {
        const u32 d0 = w_down[(4 * g + 0) * 16 + fo], d1 = w_down[(4 * g + 1) * 16 + fo];
        const u32 d2 = w_down[(4 * g + 2) * 16 + fo], d3 = w_down[(4 * g + 3) * 16 + fo];
        d = (u32x4){pack_bf(d0, d1), pack_bf(d2, d3), pack_bf(d0, d1), pack_bf(d2, d3)};
        const u32 u0 = w_up[(4 * g + 0) * 16 + fo], u1 = w_up[(4 * g + 1) * 16 + fo];
        const u32 u2 = w_up[(4 * g + 2) * 16 + fo], u3 = w_up[(4 * g + 3) * 16 + fo];
        u = (u32x4){pack_bf(u0, u1), pack_bf(u2, u3), pack_bf(u0, u1), pack_bf(u2, u3)};
    }
    w.wd = __builtin_bit_cast(bf16x8, d);
    w.wu = __builtin_bit_cast(bf16x8, u);
    const u32 s0 = w_sbf[(2 * g) * 16 + fo], s1 = w_sbf[(2 * g + 1) * 16 + fo];
    const u32 r0 = w_rbf[(2 * g) * 16 + fo], r1 = w_rbf[(2 * g + 1) * 16 + fo];
    w.ws = __builtin_bit_cast(s16x4, (u32x2){pack_bf(s0, s1), pack_bf(s0, s1)});
    w.wr = __builtin_bit_cast(s16x4, (u32x2){pack_bf(r0, r1), pack_bf(r0, r1)});
    w.cen = pack_bf(centers[2 * g], centers[2 * g + 1]);
}

__device__ __forceinline__ bf16x8 split4(float a0, float a1, float a2, float a3) {
    const u32 h0 = cvt_pk_bf16(a0, a1), h1 = cvt_pk_bf16(a2, a3);
    const u32 l0 = cvt_pk_bf16(a0 - __uint_as_float(h0 << 16), a1 - __uint_as_float(h0 & 0xffff0000u));
    const u32 l1 = cvt_pk_bf16(a2 - __uint_as_float(h1 << 16), a3 - __uint_as_float(h1 & 0xffff0000u));
    return __builtin_bit_cast(bf16x8, (u32x4){h0, h1, l0, l1});
}

__device__ __forceinline__ void epi_rows(const EpiW& w, float a0, float a1, float a2, float a3, int grow, bool valid,
                                         u32 dbits, u32x2 mv, u32x2* __restrict__ out2, int lane) {
    const int g = lane >> 4;
    const f32x4 z = {0.f, 0.f, 0.f, 0.f};
    const float d = bf2f(dbits);
    const f32x4 md = __builtin_amdgcn_mfma_f32_16x16x32_bf16(w.wd, split4(a0, a1, a2, a3), z, 0, 0, 0);
    const float x0 = d - __uint_as_float(w.cen << 16), x1 = d - __uint_as_float(w.cen & 0xffff0000u);
    const float r0 = __expf(-10.0f * x0 * x0), r1 = __expf(-10.0f * x1 * x1);
    const u32 rh = cvt_pk_bf16(r0, r1);
    const u32 rl = cvt_pk_bf16(r0 - __uint_as_float(rh << 16), r1 - __uint_as_float(rh & 0xffff0000u));
    const s16x4 rb = __builtin_bit_cast(s16x4, (u32x2){rh, rl});
    const f32x4 sg = __builtin_amdgcn_mfma_f32_16x16x16bf16_1k(w.ws, rb, z, 0, 0, 0);
    const f32x4 gt = __builtin_amdgcn_mfma_f32_16x16x16bf16_1k(w.wr, rb, z, 0, 0, 0);
    const f32x4 u = __builtin_amdgcn_mfma_f32_16x16x32_bf16(
        w.wu, split4(md[0] * sg[0], md[1] * sg[1], md[2] * sg[2], md[3] * sg[3]), z, 0, 0, 0);
    const float o0 = __builtin_fmaf(u[0], gt[0], __uint_as_float(mv[0] << 16));
    const float o1 = __builtin_fmaf(u[1], gt[1], __uint_as_float(mv[0] & 0xffff0000u));
    const float o2 = __builtin_fmaf(u[2], gt[2], __uint_as_float(mv[1] << 16));
    const float o3 = __builtin_fmaf(u[3], gt[3], __uint_as_float(mv[1] & 0xffff0000u));
    if (valid) out2[(size_t)grow * 4 + g] = (u32x2){cvt_pk_bf16(o0, o1), cvt_pk_bf16(o2, o3)};
}

__device__ __forceinline__ void decode_pair(u32 alo, u32 ahi, u32 cnt, float inv2, float& f0, float& f1) {
    const u64 ck = (u64)cnt * KBITS;
    const u64 t = (((u64)ahi << 32) | alo) - ck - (ck << 32);
    const int lo = (int)(u32)t;
    const int hi = (int)(u32)(t >> 32) + (lo < 0 ? 1 : 0);
    const float fs = (float)lo, fd = (float)hi;
    f0 = (fs + fd) * inv2;
    f1 = (fs - fd) * inv2;
}

typedef __attribute__((ext_vector_type(2))) unsigned short u16x2;

struct FE {
    u32 val[S], cumv[S];
    u32 n[S];
    u32 rounds[S];
    u32 s, r;
    u32 carry, mark;
};
struct RegSet { u32x3 d[NSB]; u32 nw, soff; bool live; };

__device__ __forceinline__ void fe_load(FE& f, RegSet& R, int lane, __amdgpu_buffer_rsrc_t rsr) {
    R.live = f.s < (u32)S;
    R.nw = 0;
    if (!R.live) return;
    const u32 k00 = f.r * CAPW;
    const u32 n0 = f.n[0];
    R.nw = min(n0 - min(n0, k00), (u32)CAPW);
    R.soff = f.s * SBYTES;
    const u32 slot = min(f.cumv[0] - k00, (u32)CAPW + lane);
    *(volatile lds_u32*)(size_t)(f.mark + slot * 4) = f.val[0];
    u32 v[NSB];
#pragma unroll
    for (int j = 0; j < NSB; ++j) v[j] = *(volatile lds_u32*)(size_t)(f.mark + (j * 64 + lane) * 4);
#pragma unroll
    for (int j = 0; j < NSB; ++j) v[j] = wave_scan_max(v[j]);
#pragma unroll
    for (int j = 0; j < NSB; ++j) {
        v[j] = max(v[j], f.carry);
        f.carry = __builtin_amdgcn_readlane(v[j], 63);
    }
#pragma unroll
    for (int j = 0; j < NSB; ++j) {
        if ((u32)(j * 64) < R.nw) {
            const u32 idx = (v[j] & 0xffffffu) + k00 + j * 64 + lane;
            R.d[j] = __builtin_amdgcn_raw_buffer_load_b96(rsr, __umul24(idx, 12u), 0, REC_AUX);
        }
    }
    f.r += 1;
    if (f.r >= f.rounds[0]) {
#pragma unroll
        for (int s = 0; s + 1 < S; ++s) { f.val[s] = f.val[s + 1]; f.cumv[s] = f.cumv[s + 1]; f.n[s] = f.n[s + 1]; f.rounds[s] = f.rounds[s + 1]; }
        f.r = 0;
        f.s += 1;
    }
}

struct Gat { u32x2 x0, x1, x2, x3; u32 la, b2; };
struct P2C { u32 q8, q16, nullo, psc2; int lane; __amdgpu_buffer_rsrc_t rsm; };

__device__ __forceinline__ Gat gat_issue(u32 lo, u32 b2, u32 soff, const P2C& k) {
    Gat g;
    const u32 ga = (lo & 0xffffu) << 5, row = lo >> 16;
    g.x0 = __builtin_amdgcn_raw_buffer_load_b64(k.rsm, quad_bcast<0>(ga) + k.q8, soff, 0);
    g.x1 = __builtin_amdgcn_raw_buffer_load_b64(k.rsm, quad_bcast<1>(ga) + k.q8, soff, 0);
    g.x2 = __builtin_amdgcn_raw_buffer_load_b64(k.rsm, quad_bcast<2>(ga) + k.q8, soff, 0);
    g.x3 = __builtin_amdgcn_raw_buffer_load_b64(k.rsm, quad_bcast<3>(ga) + k.q8, soff, 0);
    g.la = (row << 6) | ((row << 2) & 8u);
    g.b2 = b2;
    lds_add32(L2_CNT + row * 4, 1u);
    asm volatile("" : "+v"(g.la), "+v"(g.b2));
    __builtin_amdgcn_sched_barrier(0);
    return g;
}

__device__ __forceinline__ void gat_consume(const Gat& g, u32 q16) {
    auto step = [&](u32x2 x, u32 laj, u32 bj) {
        float s0, d0, s1, d1;
        asm("v_dot2_f32_bf16 %0, %1, %2, %3" : "=v"(s0) : "v"(x[0]), "v"(bj), "s"(KMAGIC));
        asm("v_dot2_f32_bf16 %0, %1, %2, %3 neg_hi:[0,1,0]" : "=v"(d0) : "v"(x[0]), "v"(bj), "s"(KMAGIC));
        asm("v_dot2_f32_bf16 %0, %1, %2, %3" : "=v"(s1) : "v"(x[1]), "v"(bj), "s"(KMAGIC));
        asm("v_dot2_f32_bf16 %0, %1, %2, %3 neg_hi:[0,1,0]" : "=v"(d1) : "v"(x[1]), "v"(bj), "s"(KMAGIC));
        const u32 a0 = laj + q16;
        lds_add64(a0, ((u64)__float_as_uint(d0) << 32) | __float_as_uint(s0));
        lds_add64(a0 ^ 8u, ((u64)__float_as_uint(d1) << 32) | __float_as_uint(s1));
    };
    step(g.x0, quad_bcast<0>(g.la), quad_bcast<0>(g.b2));
    step(g.x1, quad_bcast<1>(g.la), quad_bcast<1>(g.b2));
    step(g.x2, quad_bcast<2>(g.la), quad_bcast<2>(g.b2));
    step(g.x3, quad_bcast<3>(g.la), quad_bcast<3>(g.b2));
    __builtin_amdgcn_sched_barrier(0);
}

struct Half { u32 loa, lob, b2a, b2b; };
template <int J>
__device__ __forceinline__ Half sb_decode(const RegSet& R, const P2C& k) {
    const u32x3 d = R.d[J];
    const bool ok = (u32)(J * 64 + k.lane) < R.nw;
    const u32 cp = (d[1] & 0xffffu) | (d[2] & 0xffff0000u);
    const u32 cs = __builtin_bit_cast(u32, __builtin_bit_cast(u16x2, cp) + __builtin_bit_cast(u16x2, k.psc2));
    Half h;
    h.loa = ok ? d[0] : k.nullo;
    h.lob = ok ? __builtin_amdgcn_alignbit(d[2], d[1], 16) : k.nullo;
    h.b2a = __builtin_amdgcn_perm(cs, cs, 0x01000100u);
    h.b2b = __builtin_amdgcn_perm(cs, cs, 0x03020302u);
    return h;
}

__device__ __forceinline__ u32 slice_prefetch(u32 soff, int grp_idx, int lane, int wave, __amdgpu_buffer_rsrc_t rsm) {
    constexpr u32 LPW = (SBYTES / 128 + 511) / 512;
    static_assert(LPW <= 64, "prefetch");
    const u32 line = (grp_idx * 16 + wave) * LPW + min((u32)lane, LPW - 1);
    return __builtin_amdgcn_raw_buffer_load_b32(rsm, line * 128 + soff, 0, 0);
}

#define PF_AT 99
#define PACE_T 16
struct Pace { u32 mine, got; };
__device__ __forceinline__ void pace(Pace& p, u32 inc) {
}
template <int J>
__device__ __forceinline__ void sb_step(const RegSet& R, u32 nsb, const Half& h, const Gat& g0, const P2C& k,
                                        u32& pf, u32 pfoff, int gidx, int wave, Pace& pc) {
    pace(pc, 1);
    if constexpr (J == PF_AT) {
        if (pfoff != 0xffffffffu) {
            asm volatile("" :: "v"(pf));
            pf = slice_prefetch(pfoff, gidx, k.lane, wave, k.rsm);
        }
    }
    const Gat g1 = gat_issue(h.lob, h.b2b, R.soff, k);
    gat_consume(g0, k.q16);
    if constexpr (J + 1 < NSB) {
        if (nsb > (u32)(J + 1)) {
            const Half hn = sb_decode<J + 1>(R, k);
            const Gat gn = gat_issue(hn.loa, hn.b2a, R.soff, k);
            gat_consume(g1, k.q16);
            sb_step<J + 1>(R, nsb, hn, gn, k, pf, pfoff, gidx, wave, pc);
            return;
        }
    }
    gat_consume(g1, k.q16);
}

__global__ __launch_bounds__(1024) void p2_accumulate(const char* __restrict__ rec, const u32* __restrict__ tab,
                                                      const u32* __restrict__ meta, const u16* __restrict__ m,
                                                      const u16* __restrict__ dist, const u16* __restrict__ centers,
                                                      const u16* __restrict__ w_rbf, const u16* __restrict__ w_sbf,
                                                      const u16* __restrict__ w_down, const u16* __restrict__ w_up,
                                                      u16* __restrict__ out) {
    const int tid = threadIdx.x, lane = tid & 63;
    const int wave = __builtin_amdgcn_readfirstlane(tid >> 6);
    const int b = (blockIdx.x & 7) * 32 + (blockIdx.x >> 3);
    const int gidx = blockIdx.x >> 3;
    const auto rsr = __builtin_amdgcn_make_buffer_rsrc((void*)rec, 0, (u32)(WS_TAB), 0x00020000);
    const auto rsm = __builtin_amdgcn_make_buffer_rsrc((void*)m, 0, (u32)E_ * 32u, 0x00020000);
    const auto rst = __builtin_amdgcn_make_buffer_rsrc((void*)tab, 0, (u32)NC * NB * S * 4u, 0x00020000);
    const auto rsq = __builtin_amdgcn_make_buffer_rsrc((void*)meta, 0, 1024u, 0x00020000);
    u32 pf = slice_prefetch(0, gidx, lane, wave, rsm);
    const int c = (lane & 31) * 16 + wave;
    const u32 toff = (lane < 32 && c < NC) ? (u32)(c * NB + b) * (S * 4) : 0x80000000u;
    u32 t[S];
    {
        const u32x4 t0 = __builtin_amdgcn_raw_buffer_load_b128(rst, toff, 0, 0);
        t[0] = t0[0]; t[1] = t0[1]; t[2] = t0[2]; t[3] = t0[3];
        if constexpr (S == 5) t[4] = __builtin_amdgcn_raw_buffer_load_b32(rst, toff + 16, 0, 0);
        if constexpr (S == 6) { const u32x2 t1 = __builtin_amdgcn_raw_buffer_load_b64(rst, toff + 16, 0, 0); t[4] = t1[0]; t[S - 1] = t1[1]; }
        if constexpr (S == 8) { const u32x4 t1 = __builtin_amdgcn_raw_buffer_load_b128(rst, toff + 16, 0, 0); t[4] = t1[0]; t[5 % S] = t1[1]; t[6 % S] = t1[2]; t[7 % S] = t1[3]; }
    }
    const u32x4 mt4 = __builtin_amdgcn_raw_buffer_load_b128(rsq, lane * 16, 0, 0);
    for (u32 a = tid * 16; a < L2_SIZE; a += 1024 * 16) *(lds_u32x4*)(size_t)a = (u32x4){0u, 0u, 0u, 0u};
    const u32 mtm = max(max(mt4[0] & 0xffffu, mt4[1] & 0xffffu), max(mt4[2] & 0xffffu, mt4[3] & 0xffffu));
    const u32 mmax = __builtin_amdgcn_readlane(wave_scan_max(mtm), 63);
    const bool anynan = __builtin_amdgcn_ballot_w64(((mt4[0] | mt4[1] | mt4[2] | mt4[3]) & 0x10000u) != 0) != 0;
    const u32 eb = mmax >> 7;
    const bool fast = !anynan && (mmax == 0 || (eb >= 64 && eb <= 147));
    const u32 ebc = mmax == 0 ? 127u : min(max(eb, 64u), 147u);
    const u32 pexp = 147u - ebc;
    const float inv2 = __uint_as_float((126u - pexp) << 23);
    const int row0 = b * RB;
    const u32x2* m2 = (const u32x2*)m;
    u32x2* out2 = (u32x2*)out;
    const int e = lane & 15, g = lane >> 4;
    EpiW w;
    bool slow = !fast;
    if (fast) {
        FE f;
#pragma unroll
        for (int s = 0; s < S; ++s) {
            const u32 len = t[s] >> 16, start = t[s] & 0xffffu;
            const u32 incl = wave_scan_add(len);
            f.n[s] = __builtin_amdgcn_readlane(incl, 63);
            const u32 cum = incl - len;
            f.val[s] = (u32)c * CHP2 + start - cum + ((u32)s << 24);
            f.cumv[s] = len ? cum : 0x7fffffffu;
        }
        f.s = 0; f.r = 0; f.carry = 0; f.mark = L2_MARK + wave * ((CAPW + 64) * 4);
        P2C k;
        k.q8 = (lane & 3) * 8; k.q16 = (lane & 3) * 16; k.nullo = (u32)(RB + (lane >> 2)) << 16;
        k.psc2 = (pexp << 7) * 0x10001u; k.lane = lane; k.rsm = rsm;
        __syncthreads();
        if (lane < S) {
            u32 v = f.n[0];
#pragma unroll
            for (int s = 1; s < S; ++s) v = lane == s ? f.n[s] : v;
            *L32(L2_NW + lane * 64 + wave * 4) = v;
        }
        __syncthreads();
#pragma unroll
        for (int s = 0; s < S; ++s) {
            const u32 v = *L32(L2_NW + s * 64 + (lane & 15) * 4);
            f.rounds[s] = max((__builtin_amdgcn_readlane(wave_scan_max(v), 63) + CAPW - 1) / CAPW, 1u);
        }
        RegSet cur, nxt;
        Pace pc; pc.mine = 0; pc.got = 0;
        fe_load(f, nxt, lane, rsr);
        cur = nxt;
#pragma unroll
        for (int j = 0; j < NSB; ++j) asm volatile("" : "+v"(cur.d[j]));
        fe_load(f, nxt, lane, rsr);
        STAMP(0);
#pragma clang loop unroll(disable)
        while (cur.live) {
            const u32 nsb = (cur.nw + 63) >> 6;
            const u32 pfoff = (nxt.live && nxt.soff != cur.soff) ? nxt.soff : 0xffffffffu;
            if (nsb > 0) {
                const Half h = sb_decode<0>(cur, k);
                const Gat g0 = gat_issue(h.loa, h.b2a, cur.soff, k);
                sb_step<0>(cur, nsb, h, g0, k, pf, pfoff, gidx, wave, pc);
            }
            if (nsb < (u32)NSB) pace(pc, NSB - nsb);
            STAMP(4);
            cur = nxt;
#pragma unroll
            for (int j = 0; j < NSB; ++j) asm volatile("" : "+v"(cur.d[j]));
            STAMP(2);
            fe_load(f, nxt, lane, rsr);
            STAMP(5);
            __syncthreads();
            STAMP(1);
        }
        asm volatile("" :: "v"(pf));
        epi_weights(w, w_rbf, w_sbf, w_down, w_up, centers, lane);
        u32 dv[5]; u32x2 mv[5];
#pragma unroll
        for (int u = 0; u < 5; ++u) {
            const int r = min((wave + 16 * u) * 16 + e, RB - 1);
            dv[u] = dist[row0 + r];
            mv[u] = m2[(size_t)(row0 + r) * 4 + g];
        }
        STAMP(3);
        __syncthreads();
        STAMP(1);
        bool over = false;
#pragma unroll
        for (int u = 0; u < 5; ++u) {
            const int r = (wave + 16 * u) * 16 + e;
            const bool valid = r < RB;
            const int rc = valid ? r : RB - 1;
            const u32x4 av = *(lds_u32x4*)(size_t)(L2_ACC + rc * 64 + g * 16);
            const u32 cn = *L32(L2_CNT + rc * 4);
            over |= cn > 511u;
            const bool sw = (rc >> 1) & 1;
            float a0, a1, a2, a3;
            decode_pair(sw ? av[2] : av[0], sw ? av[3] : av[1], cn, inv2, a0, a1);
            decode_pair(sw ? av[0] : av[2], sw ? av[1] : av[3], cn, inv2, a2, a3);
            epi_rows(w, a0, a1, a2, a3, row0 + rc, valid, dv[u], mv[u], out2, lane);
        }
        if (over) *L32(L2_FLAG) = 1u;
        asm volatile("s_waitcnt vmcnt(0)" ::: "memory");
        __syncthreads();
        slow = *L32(L2_FLAG) != 0;
    }
    if (!slow) return;
    epi_weights(w, w_rbf, w_sbf, w_down, w_up, centers, lane);
    for (int half = 0; half < 2; ++half) {
        __syncthreads();
        for (u32 a = tid * 16; a < 625 * 128; a += 1024 * 16) *(lds_u32x4*)(size_t)a = (u32x4){0u, 0u, 0u, 0u};
        __syncthreads();
        for (int s = 0; s < S; ++s) {
            for (int i = 0; i < 32; ++i) {
                const int cc = i * 16 + wave;
                if (cc >= NC) break;
                const u32 tt = tab[((size_t)cc * NB + b) * S + s];
                const u32 len = (tt >> 16) * 2, start = (tt & 0xffffu) * 2;
                for (u32 j0 = 0; j0 < len; j0 += 4) {
                    const u32 j = j0 + (lane >> 4);
                    if (j < len) {
                        const u16* rp = (const u16*)(rec + ((size_t)cc * CHP2 * 2 + start + j) * 6);
                        const u32 il = rp[0], ol = rp[1], cb = rp[2];
                        const int r = (int)ol - half * 625;
                        if (r >= 0 && r < 625) {
                            const float x = bf2f(m[((size_t)s * SR + il) * 16 + (lane & 15)]);
                            __hip_atomic_fetch_add((lds_f64*)(size_t)(r * 128 + (lane & 15) * 8), (double)(bf2f(cb) * x),
                                                   __ATOMIC_RELAXED, __HIP_MEMORY_SCOPE_WORKGROUP);
                        }
                    }
                }
            }
        }
        __syncthreads();
        for (int gi = wave; gi < 40; gi += 16) {
            const int r = gi * 16 + e;
            const bool valid = r < 625;
            const int rc = valid ? r : 624;
            const int grow = row0 + half * 625 + rc;
            const lds_f64* p = (const lds_f64*)(size_t)(rc * 128 + g * 32);
            epi_rows(w, (float)p[0], (float)p[1], (float)p[2], (float)p[3], grow, valid, dist[grow], m2[(size_t)grow * 4 + g], out2, lane);
        }
    }
}

}

extern "C" void kernel_launch(void* const* d_in, const int* in_sizes, int n_in, void* d_out, int out_size, void* d_ws,
                              size_t ws_size, hipStream_t stream) {
    const u16* m = (const u16*)d_in[0];
    const int* idx_in = (const int*)d_in[1];
    const int* idx_out = (const int*)d_in[2];
    const u16* dist = (const u16*)d_in[3];
    const u16* angle = (const u16*)d_in[4];
    const u16* centers = (const u16*)d_in[5];
    const u16* ang_freq = (const u16*)d_in[6];
    const u16* w_rbf = (const u16*)d_in[7];
    const u16* w_sbf = (const u16*)d_in[8];
    const u16* w_down = (const u16*)d_in[9];
    const u16* w_up = (const u16*)d_in[10];
    char* ws = (char*)d_ws;
    (void)hipFuncSetAttribute((const void*)p1_partition, hipFuncAttributeMaxDynamicSharedMemorySize, L1_SIZE);
    (void)hipFuncSetAttribute((const void*)p2_accumulate, hipFuncAttributeMaxDynamicSharedMemorySize, L2_SIZE);
    p1_partition<<<NWG1, 1024, L1_SIZE, stream>>>((const u32x4*)idx_in, (const u32x4*)idx_out, (const u32x2*)angle, ang_freq,
                                                  (const u32x4*)m, ws + WS_REC, (u32*)(ws + WS_TAB), (u32*)(ws + WS_META));
    p2_accumulate<<<NB, 1024, L2_SIZE, stream>>>(ws + WS_REC, (const u32*)(ws + WS_TAB), (const u32*)(ws + WS_META), m, dist,
                                                 centers, w_rbf, w_sbf, w_down, w_up, (u16*)d_out);
}
```

```cpp
#include <hip/hip_runtime.h>
#include <hip/hip_bf16.h>
#include <stdint.h>

typedef unsigned int u32;
typedef unsigned long long u64;
typedef unsigned short u16;
using u16x2 = __attribute__((ext_vector_type(2))) unsigned short;
using u32x2 = __attribute__((ext_vector_type(2))) unsigned;
using u32x3 = __attribute__((ext_vector_type(3))) unsigned;
using u32x4 = __attribute__((ext_vector_type(4))) unsigned;
using f32x2 = __attribute__((ext_vector_type(2))) float;
using f32x4 = __attribute__((ext_vector_type(4))) float;
using bf16x2 = __attribute__((ext_vector_type(2))) __bf16;
using s16x4 = __attribute__((ext_vector_type(4))) short;
typedef __attribute__((address_space(3))) u64 lds_u64;
typedef __attribute__((address_space(3))) u32 lds_u32;
typedef __attribute__((address_space(3))) u16 lds_u16;
typedef __attribute__((address_space(3))) double lds_f64;
typedef __attribute__((address_space(3))) u32x2 lds_u32x2;
typedef __attribute__((address_space(3))) u32x4 lds_u32x4;

namespace {

constexpr int E_ = 320000;
constexpr int T_ = 10240000;
constexpr int NB = 256;
constexpr int RB = 1250;
constexpr int S = 5;
constexpr int SR = 65536;
constexpr u32 SBYTES = (u32)SR * 32u;
constexpr int CH = 20480;
constexpr int CHP2 = (CH + NB * S) / 2;
constexpr int NC = 500;
constexpr int NWG1 = 250;
constexpr u32 MAGIC_RB = 3435974u;
static_assert(NC * CH == T_ && NB * RB == E_ && S * SR >= E_, "shapes");

constexpr u32 NULLP = (u32)NC * CHP2;
constexpr int NNULL = 512;
constexpr size_t WS_REC = 0;
constexpr size_t WS_TAB = ((size_t)NULLP + NNULL) * 12 + 2048;
constexpr size_t WS_META = WS_TAB + (size_t)NC * NB * S * 4;
static_assert(WS_TAB % 128 == 0 && WS_META % 128 == 0, "alignment");

constexpr u32 L1_LO = 0;
constexpr u32 L1_C = CHP2 * 8;
constexpr u32 L1_H = CHP2 * 12;
constexpr u32 L1_TAB = L1_H + 16384;
constexpr u32 L1_W = L1_TAB + NB * S * 4;
constexpr u32 L1_SIZE = L1_W + 128;

constexpr int NSB = 5;
constexpr int CAPW = NSB * 64;
constexpr int CAP0 = 64;
constexpr u32 L2_CNT = 0;
constexpr u32 L2_ACC = 5120;
constexpr u32 L2_MARK = L2_ACC + (RB + 16) * 64;
constexpr u32 L2_FLAG = L2_MARK + 16 * (CAPW + 64) * 4;
constexpr u32 L2_NW = L2_FLAG + 64;
constexpr u32 L2_TABW = L2_NW + 8 * 64;
constexpr u32 L2_WD = L2_TABW + 16 * S * 256;
constexpr u32 L2_WU = L2_WD + 512;
constexpr u32 L2_WS = L2_WU + 512;
constexpr u32 L2_WR = L2_WS + 512;
constexpr u32 L2_CEN = L2_WR + 512;
constexpr u32 L2_DIST = L2_CEN + 256;
constexpr u32 L2_SIZE = L2_DIST + 2560;
static_assert(NNULL >= CAPW + 64 && NULLP + NNULL < (1u << 24), "null records");
static_assert(L2_SIZE <= 163840 && L1_SIZE <= 163840 && L2_SIZE % 16 == 0, "LDS");

constexpr int AUX_NT = 2;
constexpr int AUX_WT = 16;

constexpr float KMAGIC = 12582912.0f;
constexpr u32 KBITS = 0x4B400000u;

__device__ __forceinline__ lds_u32* L32(u32 a) { return (lds_u32*)(size_t)a; }
__device__ __forceinline__ lds_u64* L64(u32 a) { return (lds_u64*)(size_t)a; }
__device__ __forceinline__ lds_u16* L16(u32 a) { return (lds_u16*)(size_t)a; }
__device__ __forceinline__ void lds_add32(u32 a, u32 v) { __hip_atomic_fetch_add(L32(a), v, __ATOMIC_RELAXED, __HIP_MEMORY_SCOPE_WORKGROUP); }
__device__ __forceinline__ u32 lds_add32_rtn(u32 a, u32 v) { return __hip_atomic_fetch_add(L32(a), v, __ATOMIC_RELAXED, __HIP_MEMORY_SCOPE_WORKGROUP); }
__device__ __forceinline__ void lds_add64(u32 a, u64 v) { __hip_atomic_fetch_add(L64(a), v, __ATOMIC_RELAXED, __HIP_MEMORY_SCOPE_WORKGROUP); }
__device__ __forceinline__ float bf2f(u32 v) { return __uint_as_float(v << 16); }

template <int CTRL, int RM = 0xf, bool BC = true>
__device__ __forceinline__ u32 dpp0(u32 v) { return (u32)__builtin_amdgcn_update_dpp(0, (int)v, CTRL, RM, 0xf, BC); }
template <int J>
__device__ __forceinline__ u32 quad_bcast(u32 v) { return (u32)__builtin_amdgcn_mov_dpp((int)v, J * 0x55, 0xf, 0xf, true); }

__device__ __forceinline__ u32 wave_scan_add(u32 v) {
    v += dpp0<0x111>(v); v += dpp0<0x112>(v); v += dpp0<0x114>(v); v += dpp0<0x118>(v);
    v += dpp0<0x142, 0xa, false>(v); v += dpp0<0x143, 0xc, false>(v);
    return v;
}
__device__ __forceinline__ u32 wave_scan_max(u32 v) {
    v = max(v, dpp0<0x111>(v)); v = max(v, dpp0<0x112>(v)); v = max(v, dpp0<0x114>(v)); v = max(v, dpp0<0x118>(v));
    v = max(v, dpp0<0x142, 0xa, false>(v)); v = max(v, dpp0<0x143, 0xc, false>(v));
    return v;
}
__device__ __forceinline__ u32 cvt_pk_bf16(float lo, float hi) {
    return __builtin_bit_cast(u32, __builtin_convertvector((f32x2){lo, hi}, bf16x2));
}

struct Freq { float hi, lo; };
__device__ __forceinline__ Freq make_freq(float f) {
    const float C_HI = 0.15915493667125702f;
    const float C_LO = 6.4206382985757765e-09f;
    Freq r;
    r.hi = f * C_HI;
    r.lo = __builtin_fmaf(f, C_LO, __builtin_fmaf(f, C_HI, -r.hi));
    return r;
}
__device__ __forceinline__ float cos_rev(float a, const Freq& f) {
    const float n = __builtin_rintf(a * f.hi);
    float r = __builtin_fmaf(a, f.hi, -n);
    r = __builtin_fmaf(a, f.lo, r);
    return __builtin_amdgcn_cosf(r);
}


struct Raw { u32x4 o[5]; u32x4 i[5]; u32x2 a[5]; };
struct P1In { __amdgpu_buffer_rsrc_t io, ii, an; };

__device__ __forceinline__ void p1_load_group(Raw& r, int u, const P1In& in, int chunk, int tid) {
    const u32 q = chunk * (CH / 4) + u * 1024 + tid;
    r.o[u] = __builtin_amdgcn_raw_buffer_load_b128(in.io, q * 16, 0, AUX_NT);
    r.i[u] = __builtin_amdgcn_raw_buffer_load_b128(in.ii, q * 16, 0, AUX_NT);
    r.a[u] = __builtin_amdgcn_raw_buffer_load_b64(in.an, q * 8, 0, AUX_NT);
    __builtin_amdgcn_sched_barrier(0);
}

__device__ __forceinline__ void p1_convert(const Raw& raw, int u, u32 H, const Freq& freq, float& nanacc, u32* lo, u32* hi) {
#pragma unroll
    for (int j = 0; j < 4; ++j) {
        const u32 o = raw.o[u][j], i = raw.i[u][j];
        const u32 aw = raw.a[u][j >> 1];
        const float a = (j & 1) ? __uint_as_float(aw & 0xffff0000u) : __uint_as_float(aw << 16);
        const float c = cos_rev(a, freq);
        nanacc = __builtin_fmaf(c, 0.0f, nanacc);
        const u32 b = (u32)(((u64)(o & 0xffffffu) * MAGIC_RB) >> 32);
        u32 ol;
        asm("v_mad_i32_i24 %0, %1, %2, %3" : "=v"(ol) : "v"(b), "s"(-RB), "v"(o));
        lo[j] = __builtin_amdgcn_perm(ol, i, 0x05040100u);
        hi[j] = ((((i >> 8) & 0xff00u) | b) << 18) | cvt_pk_bf16(c, 0.0f);
        lds_add32(H + (hi[j] >> 16), 1u);
    }
#pragma unroll
    for (int j = 0; j < 4; ++j) asm volatile("" : "+v"(hi[j]), "+v"(lo[j]));
    __builtin_amdgcn_sched_barrier(0);
}

struct Scan { u32x2 cnt; u32 p0, p1, sum, incl; };
__device__ __forceinline__ void p1_scan_a(Scan& sc, u32 H, int tid) {
    sc.cnt = *(lds_u32x2*)(size_t)(H + tid * 8);
    sc.p0 = (sc.cnt[0] + 1) & ~1u; sc.p1 = (sc.cnt[1] + 1) & ~1u;
    sc.sum = sc.p0 + sc.p1;
    sc.incl = wave_scan_add(sc.sum);
    if ((tid & 63) == 63) *L32(L1_W + (tid >> 6) * 4) = sc.incl;
}
__device__ __forceinline__ u32 p1_scan_b(const Scan& sc, u32 H, int tid) {
    const int lane = tid & 63;
    const u32 wraw = *L32(L1_W + (lane & 15) * 4);
    const u32 wsum = wave_scan_add(lane < 16 ? wraw : 0u);
    const u32 total = __builtin_amdgcn_readlane(wsum, 15);
    const u32 woff = __builtin_amdgcn_readlane(wsum - wraw, __builtin_amdgcn_readfirstlane(tid >> 6));
    const u32 st0 = woff + sc.incl - sc.sum, st1 = st0 + sc.p0;
    *(lds_u32x2*)(size_t)(H + tid * 8) = (u32x2){st0, st1};
    if (tid < NB * S / 2) {
        const int s = (2 * tid) >> 8, b = (2 * tid) & 255;
        *L32(L1_TAB + (b * S + s) * 4) = (st0 >> 1) | (sc.p0 << 15);
        *L32(L1_TAB + ((b + 1) * S + s) * 4) = (st1 >> 1) | (sc.p1 << 15);
        if (sc.cnt[0] & 1) { *L32(L1_LO + (st0 + sc.cnt[0]) * 4) = (u32)RB << 16; *L16(L1_C + (st0 + sc.cnt[0]) * 2) = 0; }
        if (sc.cnt[1] & 1) { *L32(L1_LO + (st1 + sc.cnt[1]) * 4) = (u32)RB << 16; *L16(L1_C + (st1 + sc.cnt[1]) * 2) = 0; }
    }
    return total;
}
__device__ __forceinline__ void p1_store_tab(u32* __restrict__ tab, int chunk, int tid) {
    if (tid < NB * S / 4) {
        const auto rtb = __builtin_amdgcn_make_buffer_rsrc((void*)(tab + (size_t)chunk * NB * S), 0, (u32)NB * S * 4u, 0x00020000);
        __builtin_amdgcn_raw_buffer_store_b128(*(lds_u32x4*)(size_t)(L1_TAB + tid * 16), rtb, tid * 16, 0, AUX_WT);
    }
}
__device__ __forceinline__ void p1_copy_out(char* __restrict__ rec, int chunk, u32 total, int tid) {
    const auto rso = __builtin_amdgcn_make_buffer_rsrc((void*)(rec + (size_t)chunk * (CHP2 * 12)), 0, (u32)CHP2 * 12u, 0x00020000);
    const int npair = total >> 1;
#pragma unroll
    for (int u = 0; u < 11; ++u) {
        const int j = u * 1024 + tid;
        if (u < 10 || j < npair) {
            const u32x2 l = *(lds_u32x2*)(size_t)(L1_LO + j * 8);
            const u32 cc = *L32(L1_C + j * 4);
            const u32x3 d = {l[0], __builtin_amdgcn_perm(l[1], cc, 0x05040100u), __builtin_amdgcn_perm(cc, l[1], 0x07060302u)};
            __builtin_amdgcn_raw_buffer_store_b96(d, rso, j * 12, 0, AUX_WT);
        }
    }
}

__global__ __launch_bounds__(1024) void p1_partition(const u32x4* __restrict__ ii, const u32x4* __restrict__ io,
                                                     const u32x2* __restrict__ an, const u16* __restrict__ ang_freq,
                                                     const u32x4* __restrict__ m4, char* __restrict__ rec,
                                                     u32* __restrict__ tab, u32* __restrict__ meta) {
    constexpr u32 H0 = L1_H, H1 = L1_H + 8192;
    const int tid = threadIdx.x;
    const int c0 = blockIdx.x, c1 = blockIdx.x + NWG1;
    const u32 fbits = ang_freq[0];
    __builtin_amdgcn_sched_barrier(0);
    P1In in;
    in.io = __builtin_amdgcn_make_buffer_rsrc((void*)io, 0, (u32)T_ * 4u, 0x00020000);
    in.ii = __builtin_amdgcn_make_buffer_rsrc((void*)ii, 0, (u32)T_ * 4u, 0x00020000);
    in.an = __builtin_amdgcn_make_buffer_rsrc((void*)an, 0, (u32)T_ * 2u, 0x00020000);
    Raw raw;
#pragma unroll
    for (int u = 0; u < 5; ++u) p1_load_group(raw, u, in, c0, tid);
    *(lds_u32x4*)(size_t)(L1_H + tid * 16) = (u32x4){0u, 0u, 0u, 0u};
    if (tid < 2) *L32(L1_W + 64 + tid * 4) = 0u;
    const Freq freq = make_freq(bf2f(fbits));
    __syncthreads();
    float nanacc = 0.0f;
    u32 lo[20], hi[20];
#pragma unroll
    for (int u = 0; u < 5; ++u) {
        p1_convert(raw, u, H0, freq, nanacc, lo + 4 * u, hi + 4 * u);
        p1_load_group(raw, u, in, c1, tid);
    }
    __syncthreads();
    Scan sc;
    p1_scan_a(sc, H0, tid);
    __syncthreads();
    const u32 total0 = p1_scan_b(sc, H0, tid);
    __syncthreads();
    p1_store_tab(tab, c0, tid);
#pragma unroll
    for (int u = 0; u < 5; ++u) {
        u32 pos[4];
#pragma unroll
        for (int r = 0; r < 4; ++r) pos[r] = lds_add32_rtn(H0 + (hi[4 * u + r] >> 16), 1u);
        u32 lo1[4], hi1[4];
        p1_convert(raw, u, H1, freq, nanacc, lo1, hi1);
#pragma unroll
        for (int r = 0; r < 4; ++r) {
            *L32(L1_LO + pos[r] * 4) = lo[4 * u + r];
            *L16(L1_C + pos[r] * 2) = (u16)hi[4 * u + r];
            lo[4 * u + r] = lo1[r];
            hi[4 * u + r] = hi1[r];
        }
        __builtin_amdgcn_sched_barrier(0);
    }
    constexpr int PER = E_ * 2 / NWG1;
    u32x4 mv[3];
    mv[0] = m4[blockIdx.x * PER + tid];
    mv[1] = m4[blockIdx.x * PER + 1024 + tid];
    mv[2] = m4[blockIdx.x * PER + min(2048 + tid, PER - 1)];
    __builtin_amdgcn_sched_barrier(0);
    __syncthreads();
    p1_scan_a(sc, H1, tid);
    p1_copy_out(rec, c0, total0, tid);
    __syncthreads();
    const u32 total1 = p1_scan_b(sc, H1, tid);
    __syncthreads();
    p1_store_tab(tab, c1, tid);
#pragma unroll
    for (int r0 = 0; r0 < 20; r0 += 5) {
        u32 pos[5];
#pragma unroll
        for (int r = 0; r < 5; ++r) pos[r] = lds_add32_rtn(H1 + (hi[r0 + r] >> 16), 1u);
#pragma unroll
        for (int r = 0; r < 5; ++r) {
            *L32(L1_LO + pos[r] * 4) = lo[r0 + r];
            *L16(L1_C + pos[r] * 2) = (u16)hi[r0 + r];
        }
        __builtin_amdgcn_sched_barrier(0);
    }
    {
        u32 mx = 0;
#pragma unroll
        for (int u = 0; u < 3; ++u)
#pragma unroll
            for (int k = 0; k < 4; ++k) mx = max(mx, max(mv[u][k] & 0x7fffu, (mv[u][k] >> 16) & 0x7fffu));
        mx = wave_scan_max(mx);
        if ((tid & 63) == 63) __hip_atomic_fetch_max(L32(L1_W + 64), mx, __ATOMIC_RELAXED, __HIP_MEMORY_SCOPE_WORKGROUP);
        if (nanacc != nanacc) *L32(L1_W + 68) = 1u;
    }
    __syncthreads();
    p1_copy_out(rec, c1, total1, tid);
    if (tid == 0) meta[blockIdx.x] = *L32(L1_W + 64) | (*L32(L1_W + 68) << 16);
    if (blockIdx.x == 0 && tid >= 64 && tid < 64 + 256 - NWG1) meta[NWG1 + tid - 64] = 0u;
    if (blockIdx.x == 1 && tid < NNULL) {
        const u32 row = RB + ((tid >> 2) & 15);
        u32* p = (u32*)(rec + ((size_t)NULLP + tid) * 12);
        p[0] = row << 16; p[1] = 0u; p[2] = row;
    }
}


struct EpiW { s16x4 wd, wu, ws, wr; u32 cen; };

__device__ __forceinline__ u32 pack_bf(u32 a, u32 b) { return a | (b << 16); }

__device__ __forceinline__ void epi_weights(EpiW& w, const u16* __restrict__ w_rbf, const u16* __restrict__ w_sbf,
                                            const u16* __restrict__ w_down, const u16* __restrict__ w_up,
                                            const u16* __restrict__ centers, int lane) {
    const int fo = lane & 15, g = lane >> 4;
    w.wd = __builtin_bit_cast(s16x4, (u32x2){pack_bf(w_down[(4 * g + 0) * 16 + fo], w_down[(4 * g + 1) * 16 + fo]),
                                             pack_bf(w_down[(4 * g + 2) * 16 + fo], w_down[(4 * g + 3) * 16 + fo])});
    w.wu = __builtin_bit_cast(s16x4, (u32x2){pack_bf(w_up[(4 * g + 0) * 16 + fo], w_up[(4 * g + 1) * 16 + fo]),
                                             pack_bf(w_up[(4 * g + 2) * 16 + fo], w_up[(4 * g + 3) * 16 + fo])});
    const u32 s01 = pack_bf(w_sbf[(2 * g) * 16 + fo], w_sbf[(2 * g + 1) * 16 + fo]);
    const u32 r01 = pack_bf(w_rbf[(2 * g) * 16 + fo], w_rbf[(2 * g + 1) * 16 + fo]);
    w.ws = __builtin_bit_cast(s16x4, (u32x2){s01, s01});
    w.wr = __builtin_bit_cast(s16x4, (u32x2){r01, r01});
    w.cen = pack_bf(centers[2 * g], centers[2 * g + 1]);
}

__device__ __forceinline__ void epi_gates(const EpiW& w, u32 dbits, f32x4& sg, f32x4& gt) {
    const f32x4 z = {0.f, 0.f, 0.f, 0.f};
    const float KE = -14.426950408889634f;
    const float d = bf2f(dbits);
    const float x0 = d - __uint_as_float(w.cen << 16), x1 = d - __uint_as_float(w.cen & 0xffff0000u);
    const float r0 = __builtin_amdgcn_exp2f(x0 * KE * x0), r1 = __builtin_amdgcn_exp2f(x1 * KE * x1);
    const u32 rh = cvt_pk_bf16(r0, r1);
    const u32 rl = cvt_pk_bf16(r0 - __uint_as_float(rh << 16), r1 - __uint_as_float(rh & 0xffff0000u));
    const s16x4 rb = __builtin_bit_cast(s16x4, (u32x2){rh, rl});
    sg = __builtin_amdgcn_mfma_f32_16x16x16bf16_1k(w.ws, rb, z, 0, 0, 0);
    gt = __builtin_amdgcn_mfma_f32_16x16x16bf16_1k(w.wr, rb, z, 0, 0, 0);
}
__device__ __forceinline__ u32x2 epi_rows(const EpiW& w, float a0, float a1, float a2, float a3, float scale,
                                          const f32x4& sg, const f32x4& gt, u32x2 mv) {
    const f32x4 z = {0.f, 0.f, 0.f, 0.f};
    const s16x4 av = __builtin_bit_cast(s16x4, (u32x2){cvt_pk_bf16(a0, a1), cvt_pk_bf16(a2, a3)});
    const f32x4 md = __builtin_amdgcn_mfma_f32_16x16x16bf16_1k(w.wd, av, z, 0, 0, 0);
    const f32x4 ss = sg * scale;
    const s16x4 ag = __builtin_bit_cast(s16x4, (u32x2){cvt_pk_bf16(md[0] * ss[0], md[1] * ss[1]),
                                                       cvt_pk_bf16(md[2] * ss[2], md[3] * ss[3])});
    const f32x4 u = __builtin_amdgcn_mfma_f32_16x16x16bf16_1k(w.wu, ag, z, 0, 0, 0);
    const float o0 = __builtin_fmaf(u[0], gt[0], __uint_as_float(mv[0] << 16));
    const float o1 = __builtin_fmaf(u[1], gt[1], __uint_as_float(mv[0] & 0xffff0000u));
    const float o2 = __builtin_fmaf(u[2], gt[2], __uint_as_float(mv[1] << 16));
    const float o3 = __builtin_fmaf(u[3], gt[3], __uint_as_float(mv[1] & 0xffff0000u));
    return (u32x2){cvt_pk_bf16(o0, o1), cvt_pk_bf16(o2, o3)};
}

__device__ __forceinline__ void decode_pair(u32 alo, u32 ahi, u32 ckl, u32 ckh, float& f0, float& f1) {
    u32 borrow;
    const u32 slo = __builtin_subc(alo, ckl, 0u, &borrow);
    const u32 shi = ahi - ckh - borrow - ckl + (slo >> 31);
    const float fs = (float)(int)slo, fd = (float)(int)shi;
    f0 = fs + fd;
    f1 = fs - fd;
}


struct FE {
    u32 val, cumv;
    u32 n[S];
    u32 rounds[S];
    u32 s, r;
    u32 k00, cap;
    u32 carry, mark, tabl;
    u32 cbase;
};
struct RegSet { u32x3 d[NSB]; u32 nw, soff; bool live; };

__device__ __forceinline__ void fe_fetch(FE& f, RegSet& R, int lane, __amdgpu_buffer_rsrc_t rsr) {
    R.live = f.s < (u32)S;
    const u32 n0 = f.n[0];
    const u32 k00 = f.k00;
    R.nw = R.live ? min(n0 - min(n0, k00), f.cap) : 0u;
    R.soff = min(f.s, (u32)S - 1) * SBYTES;
    if (R.nw == 0) {
        R.d[0] = (u32x3){(u32)(RB + (lane >> 2)) << 16, 0u, (u32)(RB + (lane >> 2))};
        return;
    }
    if (f.r == 0) {
        const u32 t = *(volatile lds_u32*)(size_t)(f.tabl + f.s * 256);
        const u32 len = t >> 16, start = t & 0xffffu;
        const u32 cum = wave_scan_add(len) - len;
        f.val = (lane == 32 ? NULLP - n0 : f.cbase + start - cum) + (f.s << 24);
        f.cumv = lane == 32 ? n0 : (len ? cum : 0x7fffffffu);
    }
    const u32 slot = min(f.cumv - k00, (u32)CAPW + lane);
    *(volatile lds_u32*)(size_t)(f.mark + slot * 4) = f.val;
    u32 v[NSB];
#pragma unroll
    for (int j = 0; j < NSB; ++j) v[j] = *(volatile lds_u32*)(size_t)(f.mark + (j * 64 + lane) * 4);
#pragma unroll
    for (int j = 0; j < NSB; ++j) v[j] = wave_scan_max(v[j]);
    u32 run = f.carry;
#pragma unroll
    for (int j = 0; j < NSB; ++j) {
        v[j] = max(v[j], run);
        run = __builtin_amdgcn_readlane(v[j], 63);
        if ((u32)(j * 64 + 64) <= f.cap) f.carry = run;
    }
#pragma unroll
    for (int j = 0; j < NSB; ++j) {
        if (j == 0 || (u32)(j * 64) < R.nw) {
            const u32 idx = (v[j] & 0xffffffu) + k00 + j * 64 + lane;
            R.d[j] = __builtin_amdgcn_raw_buffer_load_b96(rsr, __umul24(idx, 12u), 0, 0);
        }
    }
}
__device__ __forceinline__ void fe_advance(FE& f) {
    if (f.s >= (u32)S) return;
    f.r += 1;
    f.k00 += f.cap;
    f.cap = CAPW;
    if (f.r >= f.rounds[0]) {
#pragma unroll
        for (int s = 0; s + 1 < S; ++s) { f.n[s] = f.n[s + 1]; f.rounds[s] = f.rounds[s + 1]; }
        f.r = 0;
        f.k00 = 0;
        f.s += 1;
    }
}

struct Gat { u32x2 x0, x1, x2, x3; u32 la, b2; };
struct P2C { u32 q8, q16, psc2; __amdgpu_buffer_rsrc_t rsm; };

__device__ __forceinline__ Gat gat_issue(u32 lo, u32 b2, u32 soff, const P2C& k) {
    Gat g;
    const u32 ga = (lo & 0xffffu) << 5, row = lo >> 16;
    g.x0 = __builtin_amdgcn_raw_buffer_load_b64(k.rsm, quad_bcast<0>(ga) + k.q8, soff, 0);
    g.x1 = __builtin_amdgcn_raw_buffer_load_b64(k.rsm, quad_bcast<1>(ga) + k.q8, soff, 0);
    g.x2 = __builtin_amdgcn_raw_buffer_load_b64(k.rsm, quad_bcast<2>(ga) + k.q8, soff, 0);
    g.x3 = __builtin_amdgcn_raw_buffer_load_b64(k.rsm, quad_bcast<3>(ga) + k.q8, soff, 0);
    g.la = (row << 6) | ((row << 2) & 8u);
    g.b2 = b2;
    lds_add32(L2_CNT + row * 4, 1u);
    asm volatile("" : "+v"(g.la), "+v"(g.b2));
    __builtin_amdgcn_sched_barrier(0);
    return g;
}

__device__ __forceinline__ void gat_consume(const Gat& g, u32 q16) {
    auto step = [&](u32x2 x, u32 laj, u32 bj) {
        float s0, d0, s1, d1;
        asm("v_dot2_f32_bf16 %0, %1, %2, %3" : "=v"(s0) : "v"(x[0]), "v"(bj), "s"(KMAGIC));
        asm("v_dot2_f32_bf16 %0, %1, %2, %3 neg_hi:[0,1,0]" : "=v"(d0) : "v"(x[0]), "v"(bj), "s"(KMAGIC));
        asm("v_dot2_f32_bf16 %0, %1, %2, %3" : "=v"(s1) : "v"(x[1]), "v"(bj), "s"(KMAGIC));
        asm("v_dot2_f32_bf16 %0, %1, %2, %3 neg_hi:[0,1,0]" : "=v"(d1) : "v"(x[1]), "v"(bj), "s"(KMAGIC));
        const u32 a0 = laj + q16;
        lds_add64(a0, ((u64)__float_as_uint(d0) << 32) | __float_as_uint(s0));
        lds_add64(a0 ^ 8u, ((u64)__float_as_uint(d1) << 32) | __float_as_uint(s1));
    };
    step(g.x0, quad_bcast<0>(g.la), quad_bcast<0>(g.b2));
    step(g.x1, quad_bcast<1>(g.la), quad_bcast<1>(g.b2));
    step(g.x2, quad_bcast<2>(g.la), quad_bcast<2>(g.b2));
    step(g.x3, quad_bcast<3>(g.la), quad_bcast<3>(g.b2));
    __builtin_amdgcn_sched_barrier(0);
}

struct Half { u32 loa, lob, b2a, b2b; };
template <int J>
__device__ __forceinline__ Half sb_decode(const RegSet& R, const P2C& k) {
    u32x3 d = R.d[J];
    asm volatile("" : "+v"(d));
    const u32 cp = (d[1] & 0xffffu) | (d[2] & 0xffff0000u);
    const u32 cs = __builtin_bit_cast(u32, __builtin_bit_cast(u16x2, cp) + __builtin_bit_cast(u16x2, k.psc2));
    Half h;
    h.loa = d[0];
    h.lob = __builtin_amdgcn_alignbit(d[2], d[1], 16);
    h.b2a = __builtin_amdgcn_perm(cs, cs, 0x01000100u);
    h.b2b = __builtin_amdgcn_perm(cs, cs, 0x03020302u);
    return h;
}

template <int J>
__device__ __forceinline__ void sb_step(const RegSet& R, u32 nsb, const Half& h, const Gat& g0, const P2C& k) {
    const Gat g1 = gat_issue(h.lob, h.b2b, R.soff, k);
    gat_consume(g0, k.q16);
    if constexpr (J + 1 < NSB) {
        if (nsb > (u32)(J + 1)) {
            const Half hn = sb_decode<J + 1>(R, k);
            if (R.nw <= (u32)((J + 1) * 64 + 32)) {
                const auto ml = __builtin_amdgcn_permlane32_swap(hn.loa, hn.lob, false, false);
                const auto mb = __builtin_amdgcn_permlane32_swap(hn.b2a, hn.b2b, false, false);
                const Gat gn = gat_issue(ml[0], mb[0], R.soff, k);
                gat_consume(g1, k.q16);
                gat_consume(gn, k.q16);
                return;
            }
            const Gat gn = gat_issue(hn.loa, hn.b2a, R.soff, k);
            gat_consume(g1, k.q16);
            sb_step<J + 1>(R, nsb, hn, gn, k);
            return;
        }
    }
    gat_consume(g1, k.q16);
}

__global__ __launch_bounds__(1024) void p2_accumulate(const char* __restrict__ rec, const u32* __restrict__ tab,
                                                      const u32* __restrict__ meta, const u16* __restrict__ m,
                                                      const u16* __restrict__ dist, const u16* __restrict__ centers,
                                                      const u16* __restrict__ w_rbf, const u16* __restrict__ w_sbf,
                                                      const u16* __restrict__ w_down, const u16* __restrict__ w_up,
                                                      u16* __restrict__ out) {
    const int tid = threadIdx.x, lane = tid & 63;
    const int wave = __builtin_amdgcn_readfirstlane(tid >> 6);
    const int b = (blockIdx.x & 7) * 32 + (blockIdx.x >> 3);
    const int row0 = b * RB;
    const auto rsr = __builtin_amdgcn_make_buffer_rsrc((void*)rec, 0, (u32)(WS_TAB), 0x00020000);
    const auto rsm = __builtin_amdgcn_make_buffer_rsrc((void*)m, 0, (u32)E_ * 32u, 0x00020000);
    const auto rst = __builtin_amdgcn_make_buffer_rsrc((void*)tab, 0, (u32)NC * NB * S * 4u, 0x00020000);
    const auto rsq = __builtin_amdgcn_make_buffer_rsrc((void*)meta, 0, 1024u, 0x00020000);
    const int c = (lane & 31) * 16 + wave;
    const u32 toff = (lane < 32 && c < NC) ? (u32)(c * NB + b) * (S * 4) : 0x80000000u;
    u32 t[S];
    {
        const u32x4 t0 = __builtin_amdgcn_raw_buffer_load_b128(rst, toff, 0, 0);
        t[0] = t0[0]; t[1] = t0[1]; t[2] = t0[2]; t[3] = t0[3];
        t[4] = __builtin_amdgcn_raw_buffer_load_b32(rst, toff + 16, 0, 0);
    }
    const u32x4 mt4 = __builtin_amdgcn_raw_buffer_load_b128(rsq, lane * 16, 0, 0);
    const u32 pf = __builtin_amdgcn_raw_buffer_load_b32(rsm, (((blockIdx.x >> 3) * 16 + wave) * 32 + (lane & 31)) * 128, 0, 0);
    __builtin_amdgcn_sched_barrier(0);
    const u32 mark = L2_MARK + wave * ((CAPW + 64) * 4);
    *(lds_u32x4*)(size_t)(mark + lane * 16) = (u32x4){0u, 0u, 0u, 0u};
    if (lane < 32) *(lds_u32x4*)(size_t)(mark + 1024 + lane * 16) = (u32x4){0u, 0u, 0u, 0u};
    static_assert((CAPW + 64) * 4 == 1536, "marker area");
    for (u32 a = tid * 16; a < L2_MARK; a += 1024 * 16) *(lds_u32x4*)(size_t)a = (u32x4){0u, 0u, 0u, 0u};
    if (tid == 0) *L32(L2_FLAG) = 0u;
    const u32 mtm = max(max(mt4[0] & 0xffffu, mt4[1] & 0xffffu), max(mt4[2] & 0xffffu, mt4[3] & 0xffffu));
    const u32 mmax = __builtin_amdgcn_readlane(wave_scan_max(mtm), 63);
    const bool anynan = __builtin_amdgcn_ballot_w64(((mt4[0] | mt4[1] | mt4[2] | mt4[3]) & 0x10000u) != 0) != 0;
    const u32 eb = mmax >> 7;
    const bool fast = !anynan && (mmax == 0 || (eb >= 64 && eb <= 147));
    const u32 ebc = mmax == 0 ? 127u : min(max(eb, 64u), 147u);
    const u32 pexp = 147u - ebc;
    const float inv2 = __uint_as_float((126u - pexp) << 23);
    const int e = lane & 15, g = lane >> 4;
    EpiW w;
    bool slow = !fast;
    if (fast) {
        FE f;
        f.tabl = L2_TABW + wave * (S * 256) + lane * 4;
        f.cbase = (u32)c * CHP2;
#pragma unroll
        for (int s = 0; s < S; ++s) {
            f.n[s] = __builtin_amdgcn_readlane(wave_scan_add(t[s] >> 16), 63);
            *L32(f.tabl + s * 256) = t[s];
        }
        f.s = 0; f.r = 0; f.carry = 0; f.mark = mark;
        f.k00 = 0; f.cap = CAP0;
        P2C k;
        k.q8 = (lane & 3) * 8; k.q16 = (lane & 3) * 16 + L2_ACC;
        k.psc2 = (pexp << 7) * 0x10001u; k.rsm = rsm;
        RegSet cur, nxt;
        fe_fetch(f, nxt, lane, rsr);
        if (lane < S) {
            u32 v = f.n[0];
#pragma unroll
            for (int s = 1; s < S; ++s) v = lane == s ? f.n[s] : v;
            *L32(L2_NW + lane * 64 + wave * 4) = v;
        }
        cur = nxt;
#pragma unroll
        for (int j = 0; j < NSB; ++j) asm volatile("" : "+v"(cur.d[j]));
        __syncthreads();
#pragma unroll
        for (int s = 0; s < S; ++s) {
            const u32 v = *L32(L2_NW + s * 64 + (lane & 15) * 4);
            const u32 mx = __builtin_amdgcn_readlane(wave_scan_max(v), 63);
            f.rounds[s] = s == 0 ? 1u + (mx - min(mx, (u32)CAP0) + CAPW - 1) / CAPW : max((mx + CAPW - 1) / CAPW, 1u);
        }
        fe_advance(f);
        Half h = sb_decode<0>(cur, k);
        Gat g0 = gat_issue(h.loa, h.b2a, cur.soff, k);
        fe_fetch(f, nxt, lane, rsr);
        fe_advance(f);
        if (wave == 0) {
            epi_weights(w, w_rbf, w_sbf, w_down, w_up, centers, lane);
            *(lds_u32x2*)(size_t)(L2_WD + lane * 8) = __builtin_bit_cast(u32x2, w.wd);
            *(lds_u32x2*)(size_t)(L2_WU + lane * 8) = __builtin_bit_cast(u32x2, w.wu);
            *(lds_u32x2*)(size_t)(L2_WS + lane * 8) = __builtin_bit_cast(u32x2, w.ws);
            *(lds_u32x2*)(size_t)(L2_WR + lane * 8) = __builtin_bit_cast(u32x2, w.wr);
            *L32(L2_CEN + lane * 4) = w.cen;
        } else if (wave < 4) {
#pragma unroll
            for (int u = 0; u < 4; ++u) {
                const int r = ((wave - 1) * 4 + u) * 128 + lane * 2;
                if (r < RB) *L32(L2_DIST + r * 2) = *(const u32*)(dist + row0 + r);
            }
        }
#pragma clang loop unroll(disable)
        while (cur.live) {
            sb_step<0>(cur, (cur.nw + 63) >> 6, h, g0, k);
            cur = nxt;
#pragma unroll
            for (int j = 0; j < NSB; ++j) asm volatile("" : "+v"(cur.d[j]));
            h = sb_decode<0>(cur, k);
            g0 = gat_issue(h.loa, h.b2a, cur.soff, k);
            fe_fetch(f, nxt, lane, rsr);
            fe_advance(f);
            __syncthreads();
        }
        asm volatile("" :: "v"(g0.x0), "v"(g0.x1), "v"(g0.x2), "v"(g0.x3), "v"(pf));
        const auto rso = __builtin_amdgcn_make_buffer_rsrc((void*)(out + (size_t)row0 * 16), 0, (u32)RB * 32u, 0x00020000);
        const auto rsi = __builtin_amdgcn_make_buffer_rsrc((void*)(m + (size_t)row0 * 16), 0, (u32)RB * 32u, 0x00020000);
        u32x2 mv[5];
        f32x4 sg[5], gt[5];
#pragma unroll
        for (int u = 0; u < 5; ++u) mv[u] = __builtin_amdgcn_raw_buffer_load_b64(rsi, ((wave + 16 * u) * 16 + e) * 32 + g * 8, 0, 0);
        w.wd = __builtin_bit_cast(s16x4, *(lds_u32x2*)(size_t)(L2_WD + lane * 8));
        w.wu = __builtin_bit_cast(s16x4, *(lds_u32x2*)(size_t)(L2_WU + lane * 8));
        w.ws = __builtin_bit_cast(s16x4, *(lds_u32x2*)(size_t)(L2_WS + lane * 8));
        w.wr = __builtin_bit_cast(s16x4, *(lds_u32x2*)(size_t)(L2_WR + lane * 8));
        w.cen = *L32(L2_CEN + lane * 4);
#pragma unroll
        for (int u = 0; u < 5; ++u) {
            const int rc = min((wave + 16 * u) * 16 + e, RB - 1);
            epi_gates(w, *L16(L2_DIST + rc * 2), sg[u], gt[u]);
        }
        u32 over = 0;
#pragma unroll
        for (int u = 0; u < 5; ++u) {
            const int r = (wave + 16 * u) * 16 + e;
            const int rc = min(r, RB - 1);
            const u32 aa = L2_ACC + rc * 64 + g * 16 + ((rc << 2) & 8);
            const u32x2 p0 = *(lds_u32x2*)(size_t)aa, p1 = *(lds_u32x2*)(size_t)(aa ^ 8);
            const u32 cn = *L32(L2_CNT + rc * 4);
            over = max(over, cn);
            const u32 q = __umul24(cn, KBITS >> 22);
            const u32 ckl = q << 22, ckh = q >> 10;
            float a0, a1, a2, a3;
            decode_pair(p0[0], p0[1], ckl, ckh, a0, a1);
            decode_pair(p1[0], p1[1], ckl, ckh, a2, a3);
            const u32x2 o = epi_rows(w, a0, a1, a2, a3, inv2, sg[u], gt[u], mv[u]);
            __builtin_amdgcn_raw_buffer_store_b64(o, rso, r * 32 + g * 8, 0, AUX_WT);
        }
        if (over > 511u) *L32(L2_FLAG) = 1u;
        asm volatile("s_waitcnt vmcnt(0)" ::: "memory");
        __syncthreads();
        slow = *L32(L2_FLAG) != 0;
    }
    if (!slow) return;
    const u32x2* m2 = (const u32x2*)m;
    u32x2* out2 = (u32x2*)out;
    epi_weights(w, w_rbf, w_sbf, w_down, w_up, centers, lane);
    for (int half = 0; half < 2; ++half) {
        __syncthreads();
        for (u32 a = tid * 16; a < 625 * 128; a += 1024 * 16) *(lds_u32x4*)(size_t)a = (u32x4){0u, 0u, 0u, 0u};
        __syncthreads();
        for (int s = 0; s < S; ++s) {
            for (int i = 0; i < 32; ++i) {
                const int cc = i * 16 + wave;
                if (cc >= NC) break;
                const u32 tt = tab[((size_t)cc * NB + b) * S + s];
                const u32 len = (tt >> 16) * 2, start = (tt & 0xffffu) * 2;
                for (u32 j0 = 0; j0 < len; j0 += 4) {
                    const u32 j = j0 + (lane >> 4);
                    if (j < len) {
                        const u16* rp = (const u16*)(rec + ((size_t)cc * CHP2 * 2 + start + j) * 6);
                        const u32 il = rp[0], ol = rp[1], cb = rp[2];
                        const int r = (int)ol - half * 625;
                        if (r >= 0 && r < 625) {
                            const float x = bf2f(m[((size_t)s * SR + il) * 16 + (lane & 15)]);
                            __hip_atomic_fetch_add((lds_f64*)(size_t)(r * 128 + (lane & 15) * 8), (double)(bf2f(cb) * x),
                                                   __ATOMIC_RELAXED, __HIP_MEMORY_SCOPE_WORKGROUP);
                        }
                    }
                }
            }
        }
        __syncthreads();
        for (int gi = wave; gi < 40; gi += 16) {
            const int r = gi * 16 + e;
            const bool valid = r < 625;
            const int rc = valid ? r : 624;
            const int grow = row0 + half * 625 + rc;
            const lds_f64* p = (const lds_f64*)(size_t)(rc * 128 + g * 32);
            f32x4 sg, gt;
            epi_gates(w, dist[grow], sg, gt);
            const u32x2 o = epi_rows(w, (float)p[0], (float)p[1], (float)p[2], (float)p[3], 1.0f, sg, gt, m2[(size_t)grow * 4 + g]);
            if (valid) out2[(size_t)grow * 4 + g] = o;
        }
    }
}

}

extern "C" void kernel_launch(void* const* d_in, const int* in_sizes, int n_in, void* d_out, int out_size, void* d_ws,
                              size_t ws_size, hipStream_t stream) {
    const u16* m = (const u16*)d_in[0];
    const int* idx_in = (const int*)d_in[1];
    const int* idx_out = (const int*)d_in[2];
    const u16* dist = (const u16*)d_in[3];
    const u16* angle = (const u16*)d_in[4];
    const u16* centers = (const u16*)d_in[5];
    const u16* ang_freq = (const u16*)d_in[6];
    const u16* w_rbf = (const u16*)d_in[7];
    const u16* w_sbf = (const u16*)d_in[8];
    const u16* w_down = (const u16*)d_in[9];
    const u16* w_up = (const u16*)d_in[10];
    char* ws = (char*)d_ws;
    (void)hipFuncSetAttribute((const void*)p1_partition, hipFuncAttributeMaxDynamicSharedMemorySize, L1_SIZE);
    (void)hipFuncSetAttribute((const void*)p2_accumulate, hipFuncAttributeMaxDynamicSharedMemorySize, L2_SIZE);
    p1_partition<<<NWG1, 1024, L1_SIZE, stream>>>((const u32x4*)idx_in, (const u32x4*)idx_out, (const u32x2*)angle, ang_freq,
                                                  (const u32x4*)m, ws + WS_REC, (u32*)(ws + WS_TAB), (u32*)(ws + WS_META));
    p2_accumulate<<<NB, 1024, L2_SIZE, stream>>>(ws + WS_REC, (const u32*)(ws + WS_TAB), (const u32*)(ws + WS_META), m, dist,
                                                 centers, w_rbf, w_sbf, w_down, w_up, (u16*)d_out);
}
```

```cpp
#include <hip/hip_runtime.h>
#include <hip/hip_bf16.h>
#include <stdint.h>

typedef unsigned int u32;
typedef unsigned long long u64;
typedef unsigned short u16;
using u16x2 = __attribute__((ext_vector_type(2))) unsigned short;
using u32x2 = __attribute__((ext_vector_type(2))) unsigned;
using u32x3 = __attribute__((ext_vector_type(3))) unsigned;
using u32x4 = __attribute__((ext_vector_type(4))) unsigned;
using f32x2 = __attribute__((ext_vector_type(2))) float;
using f32x4 = __attribute__((ext_vector_type(4))) float;
using bf16x2 = __attribute__((ext_vector_type(2))) __bf16;
using s16x4 = __attribute__((ext_vector_type(4))) short;
typedef __attribute__((address_space(3))) u64 lds_u64;
typedef __attribute__((address_space(3))) u32 lds_u32;
typedef __attribute__((address_space(3))) u16 lds_u16;
typedef __attribute__((address_space(3))) double lds_f64;
typedef __attribute__((address_space(3))) u32x2 lds_u32x2;
typedef __attribute__((address_space(3))) u32x4 lds_u32x4;

namespace {

constexpr int E_ = 320000;
constexpr int T_ = 10240000;
constexpr int NB = 256;
constexpr int RB = 1250;
constexpr int S = 5;
constexpr int SR = 65536;
constexpr u32 SBYTES = (u32)SR * 32u;
constexpr int CH = 20000;
constexpr int QLAST = CH / 4 - 4096;
constexpr int CHP2 = (CH + NB * S) / 2;
constexpr int NC = 512;
constexpr int NWG1 = 256;
constexpr u32 MAGIC_RB = 3435974u;
static_assert(NC * CH == T_ && NC == 2 * NWG1 && NC == 512 && NWG1 == 256 && NB * RB == E_ && S * SR >= E_, "shapes");

constexpr u32 NULLP = (u32)NC * CHP2;
constexpr int NNULL = 512;
constexpr size_t WS_REC = 0;
constexpr size_t WS_TAB = ((size_t)NULLP + NNULL) * 12 + 2048;
constexpr size_t WS_META = WS_TAB + (size_t)NC * NB * S * 4;
static_assert(WS_TAB % 128 == 0 && WS_META % 128 == 0, "alignment");

constexpr u32 L1_LO = 0;
constexpr u32 L1_C = CHP2 * 8;
constexpr u32 L1_H = CHP2 * 12;
constexpr u32 L1_TAB = L1_H + 16384;
constexpr u32 L1_W = L1_TAB + NB * S * 4;
constexpr u32 L1_SIZE = L1_W + 128;

constexpr int NSB = 5;
constexpr int CAPW = NSB * 64;
constexpr int CAP0 = 128;
constexpr u32 L2_CNT = 0;
constexpr u32 L2_ACC = 5120;
constexpr u32 L2_MARK = L2_ACC + (RB + 16) * 64;
constexpr u32 L2_FLAG = L2_MARK + 16 * (CAPW + 64) * 4;
constexpr u32 L2_NW = L2_FLAG + 64;
constexpr u32 L2_TABW = L2_NW + 8 * 64;
constexpr u32 L2_WD = L2_TABW + 16 * S * 256;
constexpr u32 L2_WU = L2_WD + 512;
constexpr u32 L2_WS = L2_WU + 512;
constexpr u32 L2_WR = L2_WS + 512;
constexpr u32 L2_CEN = L2_WR + 512;
constexpr u32 L2_DIST = L2_CEN + 256;
constexpr u32 L2_SIZE = L2_DIST + 2560;
static_assert(NNULL >= CAPW + 64 && NULLP + NNULL < (1u << 24), "null records");
static_assert(L2_SIZE <= 163840 && L1_SIZE <= 163840 && L2_SIZE % 16 == 0, "LDS");

constexpr int AUX_NT = 2;
constexpr int AUX_WT = 16;

constexpr float KMAGIC = 12582912.0f;
constexpr u32 KBITS = 0x4B400000u;

__device__ __forceinline__ lds_u32* L32(u32 a) { return (lds_u32*)(size_t)a; }
__device__ __forceinline__ lds_u64* L64(u32 a) { return (lds_u64*)(size_t)a; }
__device__ __forceinline__ lds_u16* L16(u32 a) { return (lds_u16*)(size_t)a; }
__device__ __forceinline__ void lds_add32(u32 a, u32 v) { __hip_atomic_fetch_add(L32(a), v, __ATOMIC_RELAXED, __HIP_MEMORY_SCOPE_WORKGROUP); }
__device__ __forceinline__ u32 lds_add32_rtn(u32 a, u32 v) { return __hip_atomic_fetch_add(L32(a), v, __ATOMIC_RELAXED, __HIP_MEMORY_SCOPE_WORKGROUP); }
__device__ __forceinline__ void lds_add64(u32 a, u64 v) { __hip_atomic_fetch_add(L64(a), v, __ATOMIC_RELAXED, __HIP_MEMORY_SCOPE_WORKGROUP); }
__device__ __forceinline__ float bf2f(u32 v) { return __uint_as_float(v << 16); }

template <int CTRL, int RM = 0xf, bool BC = true>
__device__ __forceinline__ u32 dpp0(u32 v) { return (u32)__builtin_amdgcn_update_dpp(0, (int)v, CTRL, RM, 0xf, BC); }
template <int J>
__device__ __forceinline__ u32 quad_bcast(u32 v) { return (u32)__builtin_amdgcn_mov_dpp((int)v, J * 0x55, 0xf, 0xf, true); }

__device__ __forceinline__ u32 wave_scan_add(u32 v) {
    v += dpp0<0x111>(v); v += dpp0<0x112>(v); v += dpp0<0x114>(v); v += dpp0<0x118>(v);
    v += dpp0<0x142, 0xa, false>(v); v += dpp0<0x143, 0xc, false>(v);
    return v;
}
__device__ __forceinline__ u32 wave_scan_max(u32 v) {
    v = max(v, dpp0<0x111>(v)); v = max(v, dpp0<0x112>(v)); v = max(v, dpp0<0x114>(v)); v = max(v, dpp0<0x118>(v));
    v = max(v, dpp0<0x142, 0xa, false>(v)); v = max(v, dpp0<0x143, 0xc, false>(v));
    return v;
}
__device__ __forceinline__ u32 cvt_pk_bf16(float lo, float hi) {
    return __builtin_bit_cast(u32, __builtin_convertvector((f32x2){lo, hi}, bf16x2));
}

struct Freq { float hi, lo; };
__device__ __forceinline__ Freq make_freq(float f) {
    const float C_HI = 0.15915493667125702f;
    const float C_LO = 6.4206382985757765e-09f;
    Freq r;
    r.hi = f * C_HI;
    r.lo = __builtin_fmaf(f, C_LO, __builtin_fmaf(f, C_HI, -r.hi));
    return r;
}
__device__ __forceinline__ float cos_rev(float a, const Freq& f) {
    const float n = __builtin_rintf(a * f.hi);
    float r = __builtin_fmaf(a, f.hi, -n);
    r = __builtin_fmaf(a, f.lo, r);
    return __builtin_amdgcn_cosf(r);
}


struct Raw { u32x4 o[5]; u32x4 i[5]; u32x2 a[5]; };
struct P1In { __amdgpu_buffer_rsrc_t io, ii, an; };

__device__ __forceinline__ void p1_load_group(Raw& r, int u, const P1In& in, int chunk, int tid) {
    u32 q = chunk * (CH / 4) + u * 1024 + tid;
    if (u == 4 && tid >= QLAST) q = 0x0ffffff0u;
    r.o[u] = __builtin_amdgcn_raw_buffer_load_b128(in.io, q * 16, 0, 2);
    r.i[u] = __builtin_amdgcn_raw_buffer_load_b128(in.ii, q * 16, 0, 2);
    r.a[u] = __builtin_amdgcn_raw_buffer_load_b64(in.an, q * 8, 0, 2);
    __builtin_amdgcn_sched_barrier(0);
}

__device__ __forceinline__ void p1_convert(const Raw& raw, int u, u32 H, const Freq& freq, float& nanacc, u32* lo, u32* hi) {
#pragma unroll
    for (int j = 0; j < 4; ++j) {
        const u32 o = raw.o[u][j], i = raw.i[u][j];
        const u32 aw = raw.a[u][j >> 1];
        const float a = (j & 1) ? __uint_as_float(aw & 0xffff0000u) : __uint_as_float(aw << 16);
        const float c = cos_rev(a, freq);
        nanacc = __builtin_fmaf(c, 0.0f, nanacc);
        const u32 b = (u32)(((u64)(o & 0xffffffu) * MAGIC_RB) >> 32);
        u32 ol;
        asm("v_mad_i32_i24 %0, %1, %2, %3" : "=v"(ol) : "v"(b), "s"(-RB), "v"(o));
        lo[j] = __builtin_amdgcn_perm(ol, i, 0x05040100u);
        hi[j] = ((((i >> 8) & 0xff00u) | b) << 18) | cvt_pk_bf16(c, 0.0f);
        lds_add32(H + (hi[j] >> 16), 1u);
    }
#pragma unroll
    for (int j = 0; j < 4; ++j) asm volatile("" : "+v"(hi[j]), "+v"(lo[j]));
    __builtin_amdgcn_sched_barrier(0);
}

struct Scan { u32x2 cnt; u32 p0, p1, sum, incl; };
__device__ __forceinline__ void p1_scan_a(Scan& sc, u32 H, int tid) {
    sc.cnt = *(lds_u32x2*)(size_t)(H + tid * 8);
    sc.p0 = (sc.cnt[0] + 1) & ~1u; sc.p1 = (sc.cnt[1] + 1) & ~1u;
    sc.sum = sc.p0 + sc.p1;
    sc.incl = wave_scan_add(sc.sum);
    if ((tid & 63) == 63) *L32(L1_W + (tid >> 6) * 4) = sc.incl;
}
__device__ __forceinline__ u32 p1_scan_b(const Scan& sc, u32 H, int tid) {
    const int lane = tid & 63;
    const u32 wraw = *L32(L1_W + (lane & 15) * 4);
    const u32 wsum = wave_scan_add(lane < 16 ? wraw : 0u);
    const u32 total = __builtin_amdgcn_readlane(wsum, 15);
    const u32 woff = __builtin_amdgcn_readlane(wsum - wraw, __builtin_amdgcn_readfirstlane(tid >> 6));
    const u32 st0 = woff + sc.incl - sc.sum, st1 = st0 + sc.p0;
    *(lds_u32x2*)(size_t)(H + tid * 8) = (u32x2){st0, st1};
    if (tid < NB * S / 2) {
        const int s = (2 * tid) >> 8, b = (2 * tid) & 255;
        *L32(L1_TAB + (b * S + s) * 4) = (st0 >> 1) | (sc.p0 << 15);
        *L32(L1_TAB + ((b + 1) * S + s) * 4) = (st1 >> 1) | (sc.p1 << 15);
        if (sc.cnt[0] & 1) { *L32(L1_LO + (st0 + sc.cnt[0]) * 4) = (u32)RB << 16; *L16(L1_C + (st0 + sc.cnt[0]) * 2) = 0; }
        if (sc.cnt[1] & 1) { *L32(L1_LO + (st1 + sc.cnt[1]) * 4) = (u32)RB << 16; *L16(L1_C + (st1 + sc.cnt[1]) * 2) = 0; }
    }
    return total;
}
__device__ __forceinline__ void p1_store_tab(u32* __restrict__ tab, int chunk, int tid) {
    if (tid < NB * S / 4) {
        const auto rtb = __builtin_amdgcn_make_buffer_rsrc((void*)(tab + (size_t)chunk * NB * S), 0, (u32)NB * S * 4u, 0x00020000);
        __builtin_amdgcn_raw_buffer_store_b128(*(lds_u32x4*)(size_t)(L1_TAB + tid * 16), rtb, tid * 16, 0, AUX_WT);
    }
}
__device__ __forceinline__ void p1_copy_out(char* __restrict__ rec, int chunk, u32 total, int tid) {
    const auto rso = __builtin_amdgcn_make_buffer_rsrc((void*)(rec + (size_t)chunk * (CHP2 * 12)), 0, (u32)CHP2 * 12u, 0x00020000);
    const int npair = total >> 1;
#pragma unroll
    for (int u = 0; u < 11; ++u) {
        const int j = u * 1024 + tid;
        if (u < 9 || j < npair) {
            const u32x2 l = *(lds_u32x2*)(size_t)(L1_LO + j * 8);
            const u32 cc = *L32(L1_C + j * 4);
            const u32x3 d = {l[0], __builtin_amdgcn_perm(l[1], cc, 0x05040100u), __builtin_amdgcn_perm(cc, l[1], 0x07060302u)};
            __builtin_amdgcn_raw_buffer_store_b96(d, rso, j * 12, 0, AUX_WT);
        }
    }
}

__global__ __launch_bounds__(1024) void p1_partition(const u32x4* __restrict__ ii, const u32x4* __restrict__ io,
                                                     const u32x2* __restrict__ an, const u16* __restrict__ ang_freq,
                                                     const u32x4* __restrict__ m4, char* __restrict__ rec,
                                                     u32* __restrict__ tab, u32* __restrict__ meta) {
    constexpr u32 H0 = L1_H, H1 = L1_H + 8192;
    const int tid = threadIdx.x;
    const int c0 = blockIdx.x, c1 = blockIdx.x + NWG1;
    const u32 fbits = ang_freq[0];
    __builtin_amdgcn_sched_barrier(0);
    P1In in;
    in.io = __builtin_amdgcn_make_buffer_rsrc((void*)io, 0, (u32)T_ * 4u, 0x00020000);
    in.ii = __builtin_amdgcn_make_buffer_rsrc((void*)ii, 0, (u32)T_ * 4u, 0x00020000);
    in.an = __builtin_amdgcn_make_buffer_rsrc((void*)an, 0, (u32)T_ * 2u, 0x00020000);
    Raw raw;
#pragma unroll
    for (int u = 0; u < 5; ++u) p1_load_group(raw, u, in, c0, tid);
    *(lds_u32x4*)(size_t)(L1_H + tid * 16) = (u32x4){0u, 0u, 0u, 0u};
    if (tid < 2) *L32(L1_W + 64 + tid * 4) = 0u;
    const Freq freq = make_freq(bf2f(fbits));
    __syncthreads();
    float nanacc = 0.0f;
    u32 lo[20], hi[20];
#pragma unroll
    for (int u = 0; u < 5; ++u) {
        if (u < 4 || tid < QLAST) p1_convert(raw, u, H0, freq, nanacc, lo + 4 * u, hi + 4 * u);
        p1_load_group(raw, u, in, c1, tid);
    }
    __syncthreads();
    Scan sc;
    p1_scan_a(sc, H0, tid);
    __syncthreads();
    const u32 total0 = p1_scan_b(sc, H0, tid);
    __syncthreads();
    p1_store_tab(tab, c0, tid);
#pragma unroll
    for (int u = 0; u < 5; ++u) {
        if (u < 4 || tid < QLAST) {
            u32 pos[4];
#pragma unroll
            for (int r = 0; r < 4; ++r) pos[r] = lds_add32_rtn(H0 + (hi[4 * u + r] >> 16), 1u);
            u32 lo1[4], hi1[4];
            p1_convert(raw, u, H1, freq, nanacc, lo1, hi1);
#pragma unroll
            for (int r = 0; r < 4; ++r) {
                *L32(L1_LO + pos[r] * 4) = lo[4 * u + r];
                *L16(L1_C + pos[r] * 2) = (u16)hi[4 * u + r];
                lo[4 * u + r] = lo1[r];
                hi[4 * u + r] = hi1[r];
            }
        }
        __builtin_amdgcn_sched_barrier(0);
    }
    constexpr int PER = E_ * 2 / NWG1;
    u32x4 mv[3];
    mv[0] = m4[blockIdx.x * PER + tid];
    mv[1] = m4[blockIdx.x * PER + 1024 + tid];
    mv[2] = m4[blockIdx.x * PER + min(2048 + tid, PER - 1)];
    __builtin_amdgcn_sched_barrier(0);
    __syncthreads();
    p1_scan_a(sc, H1, tid);
    p1_copy_out(rec, c0, total0, tid);
    __syncthreads();
    const u32 total1 = p1_scan_b(sc, H1, tid);
    __syncthreads();
    p1_store_tab(tab, c1, tid);
#pragma unroll
    for (int u = 0; u < 5; ++u) {
        if (u < 4 || tid < QLAST) {
            u32 pos[4];
#pragma unroll
            for (int r = 0; r < 4; ++r) pos[r] = lds_add32_rtn(H1 + (hi[4 * u + r] >> 16), 1u);
#pragma unroll
            for (int r = 0; r < 4; ++r) {
                *L32(L1_LO + pos[r] * 4) = lo[4 * u + r];
                *L16(L1_C + pos[r] * 2) = (u16)hi[4 * u + r];
            }
        }
        __builtin_amdgcn_sched_barrier(0);
    }
    {
        u32 mx = 0;
#pragma unroll
        for (int u = 0; u < 3; ++u)
#pragma unroll
            for (int k = 0; k < 4; ++k) mx = max(mx, max(mv[u][k] & 0x7fffu, (mv[u][k] >> 16) & 0x7fffu));
        mx = wave_scan_max(mx);
        if ((tid & 63) == 63) __hip_atomic_fetch_max(L32(L1_W + 64), mx, __ATOMIC_RELAXED, __HIP_MEMORY_SCOPE_WORKGROUP);
        if (nanacc != nanacc) *L32(L1_W + 68) = 1u;
    }
    __syncthreads();
    p1_copy_out(rec, c1, total1, tid);
    if (tid == 0) meta[blockIdx.x] = *L32(L1_W + 64) | (*L32(L1_W + 68) << 16);
    if (blockIdx.x == 1 && tid < NNULL) {
        const u32 row = RB + ((tid >> 2) & 15);
        u32* p = (u32*)(rec + ((size_t)NULLP + tid) * 12);
        p[0] = row << 16; p[1] = 0u; p[2] = row;
    }
}


struct EpiW { s16x4 wd, wu, ws, wr; u32 cen; };

__device__ __forceinline__ u32 pack_bf(u32 a, u32 b) { return a | (b << 16); }

__device__ __forceinline__ void epi_weights(EpiW& w, const u16* __restrict__ w_rbf, const u16* __restrict__ w_sbf,
                                            const u16* __restrict__ w_down, const u16* __restrict__ w_up,
                                            const u16* __restrict__ centers, int lane) {
    const int fo = lane & 15, g = lane >> 4;
    w.wd = __builtin_bit_cast(s16x4, (u32x2){pack_bf(w_down[(4 * g + 0) * 16 + fo], w_down[(4 * g + 1) * 16 + fo]),
                                             pack_bf(w_down[(4 * g + 2) * 16 + fo], w_down[(4 * g + 3) * 16 + fo])});
    w.wu = __builtin_bit_cast(s16x4, (u32x2){pack_bf(w_up[(4 * g + 0) * 16 + fo], w_up[(4 * g + 1) * 16 + fo]),
                                             pack_bf(w_up[(4 * g + 2) * 16 + fo], w_up[(4 * g + 3) * 16 + fo])});
    const u32 s01 = pack_bf(w_sbf[(2 * g) * 16 + fo], w_sbf[(2 * g + 1) * 16 + fo]);
    const u32 r01 = pack_bf(w_rbf[(2 * g) * 16 + fo], w_rbf[(2 * g + 1) * 16 + fo]);
    w.ws = __builtin_bit_cast(s16x4, (u32x2){s01, s01});
    w.wr = __builtin_bit_cast(s16x4, (u32x2){r01, r01});
    w.cen = pack_bf(centers[2 * g], centers[2 * g + 1]);
}

__device__ __forceinline__ void epi_gates(const EpiW& w, u32 dbits, f32x4& sg, f32x4& gt) {
    const f32x4 z = {0.f, 0.f, 0.f, 0.f};
    const float KE = -14.426950408889634f;
    const float d = bf2f(dbits);
    const float x0 = d - __uint_as_float(w.cen << 16), x1 = d - __uint_as_float(w.cen & 0xffff0000u);
    const float r0 = __builtin_amdgcn_exp2f(x0 * KE * x0), r1 = __builtin_amdgcn_exp2f(x1 * KE * x1);
    const u32 rh = cvt_pk_bf16(r0, r1);
    const u32 rl = cvt_pk_bf16(r0 - __uint_as_float(rh << 16), r1 - __uint_as_float(rh & 0xffff0000u));
    const s16x4 rb = __builtin_bit_cast(s16x4, (u32x2){rh, rl});
    sg = __builtin_amdgcn_mfma_f32_16x16x16bf16_1k(w.ws, rb, z, 0, 0, 0);
    gt = __builtin_amdgcn_mfma_f32_16x16x16bf16_1k(w.wr, rb, z, 0, 0, 0);
}
__device__ __forceinline__ u32x2 epi_rows(const EpiW& w, float a0, float a1, float a2, float a3, float scale,
                                          const f32x4& sg, const f32x4& gt, u32x2 mv) {
    const f32x4 z = {0.f, 0.f, 0.f, 0.f};
    const s16x4 av = __builtin_bit_cast(s16x4, (u32x2){cvt_pk_bf16(a0, a1), cvt_pk_bf16(a2, a3)});
    const f32x4 md = __builtin_amdgcn_mfma_f32_16x16x16bf16_1k(w.wd, av, z, 0, 0, 0);
    const f32x4 ss = sg * scale;
    const s16x4 ag = __builtin_bit_cast(s16x4, (u32x2){cvt_pk_bf16(md[0] * ss[0], md[1] * ss[1]),
                                                       cvt_pk_bf16(md[2] * ss[2], md[3] * ss[3])});
    const f32x4 u = __builtin_amdgcn_mfma_f32_16x16x16bf16_1k(w.wu, ag, z, 0, 0, 0);
    const float o0 = __builtin_fmaf(u[0], gt[0], __uint_as_float(mv[0] << 16));
    const float o1 = __builtin_fmaf(u[1], gt[1], __uint_as_float(mv[0] & 0xffff0000u));
    const float o2 = __builtin_fmaf(u[2], gt[2], __uint_as_float(mv[1] << 16));
    const float o3 = __builtin_fmaf(u[3], gt[3], __uint_as_float(mv[1] & 0xffff0000u));
    return (u32x2){cvt_pk_bf16(o0, o1), cvt_pk_bf16(o2, o3)};
}

__device__ __forceinline__ void decode_pair(u32 alo, u32 ahi, u32 ckl, u32 ckh, float& f0, float& f1) {
    u32 borrow;
    const u32 slo = __builtin_subc(alo, ckl, 0u, &borrow);
    const u32 shi = ahi - ckh - borrow - ckl + (slo >> 31);
    const float fs = (float)(int)slo, fd = (float)(int)shi;
    f0 = fs + fd;
    f1 = fs - fd;
}


struct FE {
    u32 val, cumv;
    u32 n[S];
    u32 rounds[S];
    u32 s, r;
    u32 k00, cap;
    u32 carry, mark, tabl;
    u32 cbase;
};
struct RegSet { u32x3 d[NSB]; u32 nw, soff; bool live; };

__device__ __forceinline__ void fe_fetch(FE& f, RegSet& R, int lane, __amdgpu_buffer_rsrc_t rsr) {
    R.live = f.s < (u32)S;
    const u32 n0 = f.n[0];
    const u32 k00 = f.k00;
    R.nw = R.live ? min(n0 - min(n0, k00), f.cap) : 0u;
    R.soff = min(f.s, (u32)S - 1) * SBYTES;
    if (R.nw == 0) {
        R.d[0] = (u32x3){(u32)(RB + (lane >> 2)) << 16, 0u, (u32)(RB + (lane >> 2))};
        return;
    }
    if (f.r == 0) {
        const u32 t = *(volatile lds_u32*)(size_t)(f.tabl + f.s * 256);
        const u32 len = t >> 16, start = t & 0xffffu;
        const u32 cum = wave_scan_add(len) - len;
        f.val = (lane == 32 ? NULLP - n0 : f.cbase + start - cum) + (f.s << 24);
        f.cumv = lane == 32 ? n0 : (len ? cum : 0x7fffffffu);
    }
    const u32 slot = min(f.cumv - k00, (u32)CAPW + lane);
    *(volatile lds_u32*)(size_t)(f.mark + slot * 4) = f.val;
    u32 v[NSB];
#pragma unroll
    for (int j = 0; j < NSB; ++j) v[j] = *(volatile lds_u32*)(size_t)(f.mark + (j * 64 + lane) * 4);
#pragma unroll
    for (int j = 0; j < NSB; ++j) v[j] = wave_scan_max(v[j]);
    u32 run = f.carry;
#pragma unroll
    for (int j = 0; j < NSB; ++j) {
        v[j] = max(v[j], run);
        run = __builtin_amdgcn_readlane(v[j], 63);
        if ((u32)(j * 64 + 64) <= f.cap) f.carry = run;
    }
#pragma unroll
    for (int j = 0; j < NSB; ++j) {
        if (j == 0 || (u32)(j * 64) < R.nw) {
            const u32 idx = (v[j] & 0xffffffu) + k00 + j * 64 + lane;
            R.d[j] = __builtin_amdgcn_raw_buffer_load_b96(rsr, __umul24(idx, 12u), 0, 0);
        }
    }
}
__device__ __forceinline__ void fe_advance(FE& f) {
    if (f.s >= (u32)S) return;
    f.r += 1;
    f.k00 += f.cap;
    f.cap = CAPW;
    if (f.r >= f.rounds[0]) {
#pragma unroll
        for (int s = 0; s + 1 < S; ++s) { f.n[s] = f.n[s + 1]; f.rounds[s] = f.rounds[s + 1]; }
        f.r = 0;
        f.k00 = 0;
        f.s += 1;
    }
}

struct Gat { u32x2 x0, x1, x2, x3; u32 la, b2; };
struct P2C { u32 q8, q16, psc2; __amdgpu_buffer_rsrc_t rsm; };

__device__ __forceinline__ Gat gat_issue(u32 lo, u32 b2, u32 soff, const P2C& k) {
    Gat g;
    const u32 ga = (lo & 0xffffu) << 5, row = lo >> 16;
    g.x0 = __builtin_amdgcn_raw_buffer_load_b64(k.rsm, quad_bcast<0>(ga) + k.q8, soff, 0);
    g.x1 = __builtin_amdgcn_raw_buffer_load_b64(k.rsm, quad_bcast<1>(ga) + k.q8, soff, 0);
    g.x2 = __builtin_amdgcn_raw_buffer_load_b64(k.rsm, quad_bcast<2>(ga) + k.q8, soff, 0);
    g.x3 = __builtin_amdgcn_raw_buffer_load_b64(k.rsm, quad_bcast<3>(ga) + k.q8, soff, 0);
    g.la = (row << 6) | ((row << 2) & 8u);
    g.b2 = b2;
    lds_add32(L2_CNT + row * 4, 1u);
    asm volatile("" : "+v"(g.la), "+v"(g.b2));
    __builtin_amdgcn_sched_barrier(0);
    return g;
}

__device__ __forceinline__ void gat_consume(const Gat& g, u32 q16) {
    auto step = [&](u32x2 x, u32 laj, u32 bj) {
        float s0, d0, s1, d1;
        asm("v_dot2_f32_bf16 %0, %1, %2, %3" : "=v"(s0) : "v"(x[0]), "v"(bj), "s"(KMAGIC));
        asm("v_dot2_f32_bf16 %0, %1, %2, %3 neg_hi:[0,1,0]" : "=v"(d0) : "v"(x[0]), "v"(bj), "s"(KMAGIC));
        asm("v_dot2_f32_bf16 %0, %1, %2, %3" : "=v"(s1) : "v"(x[1]), "v"(bj), "s"(KMAGIC));
        asm("v_dot2_f32_bf16 %0, %1, %2, %3 neg_hi:[0,1,0]" : "=v"(d1) : "v"(x[1]), "v"(bj), "s"(KMAGIC));
        const u32 a0 = laj + q16;
        lds_add64(a0, ((u64)__float_as_uint(d0) << 32) | __float_as_uint(s0));
        lds_add64(a0 ^ 8u, ((u64)__float_as_uint(d1) << 32) | __float_as_uint(s1));
    };
    step(g.x0, quad_bcast<0>(g.la), quad_bcast<0>(g.b2));
    step(g.x1, quad_bcast<1>(g.la), quad_bcast<1>(g.b2));
    step(g.x2, quad_bcast<2>(g.la), quad_bcast<2>(g.b2));
    step(g.x3, quad_bcast<3>(g.la), quad_bcast<3>(g.b2));
    __builtin_amdgcn_sched_barrier(0);
}

struct Half { u32 loa, lob, b2a, b2b; };
template <int J>
__device__ __forceinline__ Half sb_decode(const RegSet& R, const P2C& k) {
    u32x3 d = R.d[J];
    asm volatile("" : "+v"(d));
    const u32 cp = (d[1] & 0xffffu) | (d[2] & 0xffff0000u);
    const u32 cs = __builtin_bit_cast(u32, __builtin_bit_cast(u16x2, cp) + __builtin_bit_cast(u16x2, k.psc2));
    Half h;
    h.loa = d[0];
    h.lob = __builtin_amdgcn_alignbit(d[2], d[1], 16);
    h.b2a = __builtin_amdgcn_perm(cs, cs, 0x01000100u);
    h.b2b = __builtin_amdgcn_perm(cs, cs, 0x03020302u);
    return h;
}

template <int J>
__device__ __forceinline__ void sb_step(const RegSet& R, u32 nsb, const Half& h, const Gat& g0, const P2C& k) {
    const Gat g1 = gat_issue(h.lob, h.b2b, R.soff, k);
    gat_consume(g0, k.q16);
    if constexpr (J + 1 < NSB) {
        if (nsb > (u32)(J + 1)) {
            const Half hn = sb_decode<J + 1>(R, k);
            if (R.nw <= (u32)((J + 1) * 64 + 32)) {
                const auto ml = __builtin_amdgcn_permlane32_swap(hn.loa, hn.lob, false, false);
                const auto mb = __builtin_amdgcn_permlane32_swap(hn.b2a, hn.b2b, false, false);
                const Gat gn = gat_issue(ml[0], mb[0], R.soff, k);
                gat_consume(g1, k.q16);
                gat_consume(gn, k.q16);
                return;
            }
            const Gat gn = gat_issue(hn.loa, hn.b2a, R.soff, k);
            gat_consume(g1, k.q16);
            sb_step<J + 1>(R, nsb, hn, gn, k);
            return;
        }
    }
    gat_consume(g1, k.q16);
}

__global__ __launch_bounds__(1024) void p2_accumulate(const char* __restrict__ rec, const u32* __restrict__ tab,
                                                      const u32* __restrict__ meta, const u16* __restrict__ m,
                                                      const u16* __restrict__ dist, const u16* __restrict__ centers,
                                                      const u16* __restrict__ w_rbf, const u16* __restrict__ w_sbf,
                                                      const u16* __restrict__ w_down, const u16* __restrict__ w_up,
                                                      u16* __restrict__ out) {
    const int tid = threadIdx.x, lane = tid & 63;
    const int wave = __builtin_amdgcn_readfirstlane(tid >> 6);
    const int b = (blockIdx.x & 7) * 32 + (blockIdx.x >> 3);
    const int row0 = b * RB;
    const auto rsr = __builtin_amdgcn_make_buffer_rsrc((void*)rec, 0, (u32)(WS_TAB), 0x00020000);
    const auto rsm = __builtin_amdgcn_make_buffer_rsrc((void*)m, 0, (u32)E_ * 32u, 0x00020000);
    const auto rst = __builtin_amdgcn_make_buffer_rsrc((void*)tab, 0, (u32)NC * NB * S * 4u, 0x00020000);
    const auto rsq = __builtin_amdgcn_make_buffer_rsrc((void*)meta, 0, 1024u, 0x00020000);
    const int c = (lane & 31) * 16 + wave;
    const u32 toff = lane < 32 ? (u32)(c * NB + b) * (S * 4) : 0x80000000u;
    u32 t[S];
    {
        const u32x4 t0 = __builtin_amdgcn_raw_buffer_load_b128(rst, toff, 0, 0);
        t[0] = t0[0]; t[1] = t0[1]; t[2] = t0[2]; t[3] = t0[3];
        t[4] = __builtin_amdgcn_raw_buffer_load_b32(rst, toff + 16, 0, 0);
    }
    const u32x4 mt4 = __builtin_amdgcn_raw_buffer_load_b128(rsq, lane * 16, 0, 0);
    __builtin_amdgcn_sched_barrier(0);
    const u32 mark = L2_MARK + wave * ((CAPW + 64) * 4);
    *(lds_u32x4*)(size_t)(mark + lane * 16) = (u32x4){0u, 0u, 0u, 0u};
    if (lane < 32) *(lds_u32x4*)(size_t)(mark + 1024 + lane * 16) = (u32x4){0u, 0u, 0u, 0u};
    static_assert((CAPW + 64) * 4 == 1536, "marker area");
    for (u32 a = tid * 16; a < L2_MARK; a += 1024 * 16) *(lds_u32x4*)(size_t)a = (u32x4){0u, 0u, 0u, 0u};
    if (tid == 0) *L32(L2_FLAG) = 0u;
    const u32 mtm = max(max(mt4[0] & 0xffffu, mt4[1] & 0xffffu), max(mt4[2] & 0xffffu, mt4[3] & 0xffffu));
    const u32 mmax = __builtin_amdgcn_readlane(wave_scan_max(mtm), 63);
    const bool anynan = __builtin_amdgcn_ballot_w64(((mt4[0] | mt4[1] | mt4[2] | mt4[3]) & 0x10000u) != 0) != 0;
    const u32 eb = mmax >> 7;
    const bool fast = !anynan && (mmax == 0 || (eb >= 64 && eb <= 147));
    const u32 ebc = mmax == 0 ? 127u : min(max(eb, 64u), 147u);
    const u32 pexp = 147u - ebc;
    const float inv2 = __uint_as_float((126u - pexp) << 23);
    const int e = lane & 15, g = lane >> 4;
    EpiW w;
    bool slow = !fast;
    if (fast) {
        FE f;
        f.tabl = L2_TABW + wave * (S * 256) + lane * 4;
        f.cbase = (u32)c * CHP2;
#pragma unroll
        for (int s = 0; s < S; ++s) {
            f.n[s] = __builtin_amdgcn_readlane(wave_scan_add(t[s] >> 16), 63);
            *L32(f.tabl + s * 256) = t[s];
        }
        f.s = 0; f.r = 0; f.carry = 0; f.mark = mark;
        f.k00 = 0; f.cap = CAP0;
        P2C k;
        k.q8 = (lane & 3) * 8; k.q16 = (lane & 3) * 16 + L2_ACC;
        k.psc2 = (pexp << 7) * 0x10001u; k.rsm = rsm;
        RegSet cur, nxt;
        fe_fetch(f, nxt, lane, rsr);
        if (lane < S) {
            u32 v = f.n[0];
#pragma unroll
            for (int s = 1; s < S; ++s) v = lane == s ? f.n[s] : v;
            *L32(L2_NW + lane * 64 + wave * 4) = v;
        }
        cur = nxt;
#pragma unroll
        for (int j = 0; j < NSB; ++j) asm volatile("" : "+v"(cur.d[j]));
        __syncthreads();
#pragma unroll
        for (int s = 0; s < S; ++s) {
            const u32 v = *L32(L2_NW + s * 64 + (lane & 15) * 4);
            const u32 mx = __builtin_amdgcn_readlane(wave_scan_max(v), 63);
            f.rounds[s] = s == 0 ? 1u + (mx - min(mx, (u32)CAP0) + CAPW - 1) / CAPW : max((mx + CAPW - 1) / CAPW, 1u);
        }
        fe_advance(f);
        Half h = sb_decode<0>(cur, k);
        Gat g0 = gat_issue(h.loa, h.b2a, cur.soff, k);
        fe_fetch(f, nxt, lane, rsr);
        fe_advance(f);
        if (wave == 0) {
            epi_weights(w, w_rbf, w_sbf, w_down, w_up, centers, lane);
            *(lds_u32x2*)(size_t)(L2_WD + lane * 8) = __builtin_bit_cast(u32x2, w.wd);
            *(lds_u32x2*)(size_t)(L2_WU + lane * 8) = __builtin_bit_cast(u32x2, w.wu);
            *(lds_u32x2*)(size_t)(L2_WS + lane * 8) = __builtin_bit_cast(u32x2, w.ws);
            *(lds_u32x2*)(size_t)(L2_WR + lane * 8) = __builtin_bit_cast(u32x2, w.wr);
            *L32(L2_CEN + lane * 4) = w.cen;
        } else if (wave < 4) {
#pragma unroll
            for (int u = 0; u < 4; ++u) {
                const int r = ((wave - 1) * 4 + u) * 128 + lane * 2;
                if (r < RB) *L32(L2_DIST + r * 2) = *(const u32*)(dist + row0 + r);
            }
        }
#pragma clang loop unroll(disable)
        while (cur.live) {
            sb_step<0>(cur, (cur.nw + 63) >> 6, h, g0, k);
            cur = nxt;
#pragma unroll
            for (int j = 0; j < NSB; ++j) asm volatile("" : "+v"(cur.d[j]));
            h = sb_decode<0>(cur, k);
            g0 = gat_issue(h.loa, h.b2a, cur.soff, k);
            fe_fetch(f, nxt, lane, rsr);
            fe_advance(f);
            __syncthreads();
        }
        asm volatile("" :: "v"(g0.x0), "v"(g0.x1), "v"(g0.x2), "v"(g0.x3));
        const auto rso = __builtin_amdgcn_make_buffer_rsrc((void*)(out + (size_t)row0 * 16), 0, (u32)RB * 32u, 0x00020000);
        const auto rsi = __builtin_amdgcn_make_buffer_rsrc((void*)(m + (size_t)row0 * 16), 0, (u32)RB * 32u, 0x00020000);
        u32x2 mv[5];
        f32x4 sg[5], gt[5];
#pragma unroll
        for (int u = 0; u < 5; ++u) mv[u] = __builtin_amdgcn_raw_buffer_load_b64(rsi, ((wave + 16 * u) * 16 + e) * 32 + g * 8, 0, 0);
        w.wd = __builtin_bit_cast(s16x4, *(lds_u32x2*)(size_t)(L2_WD + lane * 8));
        w.wu = __builtin_bit_cast(s16x4, *(lds_u32x2*)(size_t)(L2_WU + lane * 8));
        w.ws = __builtin_bit_cast(s16x4, *(lds_u32x2*)(size_t)(L2_WS + lane * 8));
        w.wr = __builtin_bit_cast(s16x4, *(lds_u32x2*)(size_t)(L2_WR + lane * 8));
        w.cen = *L32(L2_CEN + lane * 4);
#pragma unroll
        for (int u = 0; u < 5; ++u) {
            const int rc = min((wave + 16 * u) * 16 + e, RB - 1);
            epi_gates(w, *L16(L2_DIST + rc * 2), sg[u], gt[u]);
        }
        u32 over = 0;
#pragma unroll
        for (int u = 0; u < 5; ++u) {
            const int r = (wave + 16 * u) * 16 + e;
            const int rc = min(r, RB - 1);
            const u32 aa = L2_ACC + rc * 64 + g * 16 + ((rc << 2) & 8);
            const u32x2 p0 = *(lds_u32x2*)(size_t)aa, p1 = *(lds_u32x2*)(size_t)(aa ^ 8);
            const u32 cn = *L32(L2_CNT + rc * 4);
            over = max(over, cn);
            const u32 q = __umul24(cn, KBITS >> 22);
            const u32 ckl = q << 22, ckh = q >> 10;
            float a0, a1, a2, a3;
            decode_pair(p0[0], p0[1], ckl, ckh, a0, a1);
            decode_pair(p1[0], p1[1], ckl, ckh, a2, a3);
            const u32x2 o = epi_rows(w, a0, a1, a2, a3, inv2, sg[u], gt[u], mv[u]);
            __builtin_amdgcn_raw_buffer_store_b64(o, rso, r * 32 + g * 8, 0, AUX_WT);
        }
        if (over > 511u) *L32(L2_FLAG) = 1u;
        asm volatile("s_waitcnt vmcnt(0)" ::: "memory");
        __syncthreads();
        slow = *L32(L2_FLAG) != 0;
    }
    if (!slow) return;
    const u32x2* m2 = (const u32x2*)m;
    u32x2* out2 = (u32x2*)out;
    epi_weights(w, w_rbf, w_sbf, w_down, w_up, centers, lane);
    for (int half = 0; half < 2; ++half) {
        __syncthreads();
        for (u32 a = tid * 16; a < 625 * 128; a += 1024 * 16) *(lds_u32x4*)(size_t)a = (u32x4){0u, 0u, 0u, 0u};
        __syncthreads();
        for (int s = 0; s < S; ++s) {
            for (int i = 0; i < 32; ++i) {
                const int cc = i * 16 + wave;
                const u32 tt = tab[((size_t)cc * NB + b) * S + s];
                const u32 len = (tt >> 16) * 2, start = (tt & 0xffffu) * 2;
                for (u32 j0 = 0; j0 < len; j0 += 4) {
                    const u32 j = j0 + (lane >> 4);
                    if (j < len) {
                        const u16* rp = (const u16*)(rec + ((size_t)cc * CHP2 * 2 + start + j) * 6);
                        const u32 il = rp[0], ol = rp[1], cb = rp[2];
                        const int r = (int)ol - half * 625;
                        if (r >= 0 && r < 625) {
                            const float x = bf2f(m[((size_t)s * SR + il) * 16 + (lane & 15)]);
                            __hip_atomic_fetch_add((lds_f64*)(size_t)(r * 128 + (lane & 15) * 8), (double)(bf2f(cb) * x),
                                                   __ATOMIC_RELAXED, __HIP_MEMORY_SCOPE_WORKGROUP);
                        }
                    }
                }
            }
        }
        __syncthreads();
        for (int gi = wave; gi < 40; gi += 16) {
            const int r = gi * 16 + e;
            const bool valid = r < 625;
            const int rc = valid ? r : 624;
            const int grow = row0 + half * 625 + rc;
            const lds_f64* p = (const lds_f64*)(size_t)(rc * 128 + g * 32);
            f32x4 sg, gt;
            epi_gates(w, dist[grow], sg, gt);
            const u32x2 o = epi_rows(w, (float)p[0], (float)p[1], (float)p[2], (float)p[3], 1.0f, sg, gt, m2[(size_t)grow * 4 + g]);
            if (valid) out2[(size_t)grow * 4 + g] = o;
        }
    }
}

}

extern "C" void kernel_launch(void* const* d_in, const int* in_sizes, int n_in, void* d_out, int out_size, void* d_ws,
                              size_t ws_size, hipStream_t stream) {
    const u16* m = (const u16*)d_in[0];
    const int* idx_in = (const int*)d_in[1];
    const int* idx_out = (const int*)d_in[2];
    const u16* dist = (const u16*)d_in[3];
    const u16* angle = (const u16*)d_in[4];
    const u16* centers = (const u16*)d_in[5];
    const u16* ang_freq = (const u16*)d_in[6];
    const u16* w_rbf = (const u16*)d_in[7];
    const u16* w_sbf = (const u16*)d_in[8];
    const u16* w_down = (const u16*)d_in[9];
    const u16* w_up = (const u16*)d_in[10];
    char* ws = (char*)d_ws;
    (void)hipFuncSetAttribute((const void*)p1_partition, hipFuncAttributeMaxDynamicSharedMemorySize, L1_SIZE);
    (void)hipFuncSetAttribute((const void*)p2_accumulate, hipFuncAttributeMaxDynamicSharedMemorySize, L2_SIZE);
    p1_partition<<<NWG1, 1024, L1_SIZE, stream>>>((const u32x4*)idx_in, (const u32x4*)idx_out, (const u32x2*)angle, ang_freq,
                                                  (const u32x4*)m, ws + WS_REC, (u32*)(ws + WS_TAB), (u32*)(ws + WS_META));
    p2_accumulate<<<NB, 1024, L2_SIZE, stream>>>(ws + WS_REC, (const u32*)(ws + WS_TAB), (const u32*)(ws + WS_META), m, dist,
                                                 centers, w_rbf, w_sbf, w_down, w_up, (u16*)d_out);
}
```

```cpp
#include <hip/hip_runtime.h>
#include <hip/hip_bf16.h>
#include <stdint.h>

typedef unsigned int u32;
typedef unsigned long long u64;
typedef unsigned short u16;
using u16x2 = __attribute__((ext_vector_type(2))) unsigned short;
using u32x2 = __attribute__((ext_vector_type(2))) unsigned;
using u32x3 = __attribute__((ext_vector_type(3))) unsigned;
using u32x4 = __attribute__((ext_vector_type(4))) unsigned;
using f32x2 = __attribute__((ext_vector_type(2))) float;
using f32x4 = __attribute__((ext_vector_type(4))) float;
using bf16x2 = __attribute__((ext_vector_type(2))) __bf16;
using s16x4 = __attribute__((ext_vector_type(4))) short;
typedef __attribute__((address_space(3))) u64 lds_u64;
typedef __attribute__((address_space(3))) u32 lds_u32;
typedef __attribute__((address_space(3))) u16 lds_u16;
typedef __attribute__((address_space(3))) double lds_f64;
typedef __attribute__((address_space(3))) u32x2 lds_u32x2;
typedef __attribute__((address_space(3))) u32x4 lds_u32x4;

namespace {

constexpr int E_ = 320000;
constexpr int T_ = 10240000;
constexpr int NB = 256;
constexpr int RB = 1250;
constexpr int S = 5;
constexpr int SR = 65536;
constexpr u32 SBYTES = (u32)SR * 32u;
constexpr int CH = 20000;
constexpr int QLAST = CH / 4 - 4096;
constexpr int CHP2 = (CH + NB * S) / 2;
constexpr int NC = 512;
constexpr int NWG1 = 256;
constexpr u32 MAGIC_RB = 3435974u;
static_assert(NC * CH == T_ && NC == 2 * NWG1 && NC == 512 && NWG1 == 256 && NB * RB == E_ && S * SR >= E_, "shapes");

constexpr u32 NULLP = (u32)NC * CHP2;
constexpr int NNULL = 512;
constexpr size_t WS_REC = 0;
constexpr size_t WS_TAB = ((size_t)NULLP + NNULL) * 12 + 2048;
constexpr size_t WS_META = WS_TAB + (size_t)NC * NB * S * 4;
static_assert(WS_TAB % 128 == 0 && WS_META % 128 == 0, "alignment");

constexpr u32 L1_LO = 0;
constexpr u32 L1_C = CHP2 * 8;
constexpr u32 L1_H = CHP2 * 12;
constexpr u32 L1_TAB = L1_H + 16384;
constexpr u32 L1_W = L1_TAB + NB * S * 4;
constexpr u32 L1_SIZE = L1_W + 128;

constexpr int NSB = 5;
constexpr int CAPW = NSB * 64;
constexpr int CAP0 = 128;
constexpr u32 L2_CNT = 0;
constexpr u32 L2_ACC = 5120;
constexpr u32 L2_MARK = L2_ACC + (RB + 16) * 64;
constexpr u32 L2_FLAG = L2_MARK + 16 * (CAPW + 64) * 4;
constexpr u32 L2_NW = L2_FLAG + 64;
constexpr u32 L2_TABW = L2_NW + 8 * 64;
constexpr u32 L2_WD = L2_TABW + 16 * S * 256;
constexpr u32 L2_WU = L2_WD + 512;
constexpr u32 L2_WS = L2_WU + 512;
constexpr u32 L2_WR = L2_WS + 512;
constexpr u32 L2_CEN = L2_WR + 512;
constexpr u32 L2_DIST = L2_CEN + 256;
constexpr u32 L2_SIZE = L2_DIST + 2560;
static_assert(NNULL >= CAPW + 64 && NULLP + NNULL < (1u << 24), "null records");
static_assert(L2_SIZE <= 163840 && L1_SIZE <= 163840 && L2_SIZE % 16 == 0, "LDS");

constexpr int AUX_NT = 2;
constexpr int AUX_WT = 16;

constexpr float KMAGIC = 12582912.0f;
constexpr u32 KBITS = 0x4B400000u;

__device__ __forceinline__ lds_u32* L32(u32 a) { return (lds_u32*)(size_t)a; }
__device__ __forceinline__ lds_u64* L64(u32 a) { return (lds_u64*)(size_t)a; }
__device__ __forceinline__ lds_u16* L16(u32 a) { return (lds_u16*)(size_t)a; }
__device__ __forceinline__ void lds_add32(u32 a, u32 v) { __hip_atomic_fetch_add(L32(a), v, __ATOMIC_RELAXED, __HIP_MEMORY_SCOPE_WORKGROUP); }
__device__ __forceinline__ u32 lds_add32_rtn(u32 a, u32 v) { return __hip_atomic_fetch_add(L32(a), v, __ATOMIC_RELAXED, __HIP_MEMORY_SCOPE_WORKGROUP); }
__device__ __forceinline__ void lds_add64(u32 a, u64 v) { __hip_atomic_fetch_add(L64(a), v, __ATOMIC_RELAXED, __HIP_MEMORY_SCOPE_WORKGROUP); }
__device__ __forceinline__ float bf2f(u32 v) { return __uint_as_float(v << 16); }

template <int CTRL, int RM = 0xf, bool BC = true>
__device__ __forceinline__ u32 dpp0(u32 v) { return (u32)__builtin_amdgcn_update_dpp(0, (int)v, CTRL, RM, 0xf, BC); }
template <int J>
__device__ __forceinline__ u32 quad_bcast(u32 v) { return (u32)__builtin_amdgcn_mov_dpp((int)v, J * 0x55, 0xf, 0xf, true); }

__device__ __forceinline__ u32 wave_scan_add(u32 v) {
    v += dpp0<0x111>(v); v += dpp0<0x112>(v); v += dpp0<0x114>(v); v += dpp0<0x118>(v);
    v += dpp0<0x142, 0xa, false>(v); v += dpp0<0x143, 0xc, false>(v);
    return v;
}
__device__ __forceinline__ u32 wave_scan_max(u32 v) {
    v = max(v, dpp0<0x111>(v)); v = max(v, dpp0<0x112>(v)); v = max(v, dpp0<0x114>(v)); v = max(v, dpp0<0x118>(v));
    v = max(v, dpp0<0x142, 0xa, false>(v)); v = max(v, dpp0<0x143, 0xc, false>(v));
    return v;
}
__device__ __forceinline__ u32 cvt_pk_bf16(float lo, float hi) {
    return __builtin_bit_cast(u32, __builtin_convertvector((f32x2){lo, hi}, bf16x2));
}

struct Freq { float hi, lo; };
__device__ __forceinline__ Freq make_freq(float f) {
    const float C_HI = 0.15915493667125702f;
    const float C_LO = 6.4206382985757765e-09f;
    Freq r;
    r.hi = f * C_HI;
    r.lo = __builtin_fmaf(f, C_LO, __builtin_fmaf(f, C_HI, -r.hi));
    return r;
}
__device__ __forceinline__ float cos_rev(float a, const Freq& f) {
    const float n = __builtin_rintf(a * f.hi);
    float r = __builtin_fmaf(a, f.hi, -n);
    r = __builtin_fmaf(a, f.lo, r);
    return __builtin_amdgcn_cosf(r);
}


struct Raw { u32x4 o[5]; u32x4 i[5]; u32x2 a[5]; };
struct P1In { __amdgpu_buffer_rsrc_t io, ii, an; };

__device__ __forceinline__ void p1_load_group(Raw& r, int u, const P1In& in, int chunk, int tid) {
    u32 q = chunk * (CH / 4) + u * 1024 + tid;
    if (u == 4 && tid >= QLAST) q = 0x0ffffff0u;
    r.o[u] = __builtin_amdgcn_raw_buffer_load_b128(in.io, q * 16, 0, AUX_NT);
    r.i[u] = __builtin_amdgcn_raw_buffer_load_b128(in.ii, q * 16, 0, AUX_NT);
    r.a[u] = __builtin_amdgcn_raw_buffer_load_b64(in.an, q * 8, 0, AUX_NT);
    __builtin_amdgcn_sched_barrier(0);
}

__device__ __forceinline__ void p1_convert(const Raw& raw, int u, u32 H, const Freq& freq, float& nanacc, u32* lo, u32* hi) {
#pragma unroll
    for (int j = 0; j < 4; ++j) {
        const u32 o = raw.o[u][j], i = raw.i[u][j];
        const u32 aw = raw.a[u][j >> 1];
        const float a = (j & 1) ? __uint_as_float(aw & 0xffff0000u) : __uint_as_float(aw << 16);
        const float c = cos_rev(a, freq);
        nanacc = __builtin_fmaf(c, 0.0f, nanacc);
        const u32 b = (u32)(((u64)(o & 0xffffffu) * MAGIC_RB) >> 32);
        u32 ol;
        asm("v_mad_i32_i24 %0, %1, %2, %3" : "=v"(ol) : "v"(b), "s"(-RB), "v"(o));
        lo[j] = __builtin_amdgcn_perm(ol, i, 0x05040100u);
        hi[j] = ((((i >> 8) & 0xff00u) | b) << 18) | cvt_pk_bf16(c, 0.0f);
        lds_add32(H + (hi[j] >> 16), 1u);
    }
#pragma unroll
    for (int j = 0; j < 4; ++j) asm volatile("" : "+v"(hi[j]), "+v"(lo[j]));
    __builtin_amdgcn_sched_barrier(0);
}

struct Scan { u32x2 cnt; u32 p0, p1, sum, incl; };
__device__ __forceinline__ void p1_scan_a(Scan& sc, u32 H, int tid) {
    sc.cnt = *(lds_u32x2*)(size_t)(H + tid * 8);
    sc.p0 = (sc.cnt[0] + 1) & ~1u; sc.p1 = (sc.cnt[1] + 1) & ~1u;
    sc.sum = sc.p0 + sc.p1;
    sc.incl = wave_scan_add(sc.sum);
    if ((tid & 63) == 63) *L32(L1_W + (tid >> 6) * 4) = sc.incl;
}
__device__ __forceinline__ u32 p1_scan_b(const Scan& sc, u32 H, int tid) {
    const int lane = tid & 63;
    const u32 wraw = *L32(L1_W + (lane & 15) * 4);
    const u32 wsum = wave_scan_add(lane < 16 ? wraw : 0u);
    const u32 total = __builtin_amdgcn_readlane(wsum, 15);
    const u32 woff = __builtin_amdgcn_readlane(wsum - wraw, __builtin_amdgcn_readfirstlane(tid >> 6));
    const u32 st0 = woff + sc.incl - sc.sum, st1 = st0 + sc.p0;
    *(lds_u32x2*)(size_t)(H + tid * 8) = (u32x2){st0, st1};
    if (tid < NB * S / 2) {
        const int s = (2 * tid) >> 8, b = (2 * tid) & 255;
        *L32(L1_TAB + (b * S + s) * 4) = (st0 >> 1) | (sc.p0 << 15);
        *L32(L1_TAB + ((b + 1) * S + s) * 4) = (st1 >> 1) | (sc.p1 << 15);
        if (sc.cnt[0] & 1) { *L32(L1_LO + (st0 + sc.cnt[0]) * 4) = (u32)RB << 16; *L16(L1_C + (st0 + sc.cnt[0]) * 2) = 0; }
        if (sc.cnt[1] & 1) { *L32(L1_LO + (st1 + sc.cnt[1]) * 4) = (u32)RB << 16; *L16(L1_C + (st1 + sc.cnt[1]) * 2) = 0; }
    }
    return total;
}
__device__ __forceinline__ void p1_store_tab(u32* __restrict__ tab, int chunk, int tid) {
    if (tid < NB * S / 4) {
        const auto rtb = __builtin_amdgcn_make_buffer_rsrc((void*)(tab + (size_t)chunk * NB * S), 0, (u32)NB * S * 4u, 0x00020000);
        __builtin_amdgcn_raw_buffer_store_b128(*(lds_u32x4*)(size_t)(L1_TAB + tid * 16), rtb, tid * 16, 0, AUX_WT);
    }
}
constexpr int NOUT = (CHP2 + 1023) / 1024;
struct Out { u32x3 d[NOUT]; };
__device__ __forceinline__ void p1_out_read(Out& o, int tid) {
#pragma unroll
    for (int u = 0; u < NOUT; ++u) {
        const int j = u * 1024 + tid;
        const u32x2 l = *(lds_u32x2*)(size_t)(L1_LO + j * 8);
        const u32 cc = *L32(L1_C + j * 4);
        o.d[u] = (u32x3){l[0], __builtin_amdgcn_perm(l[1], cc, 0x05040100u), __builtin_amdgcn_perm(cc, l[1], 0x07060302u)};
    }
}
__device__ __forceinline__ void p1_out_store(const Out& o, int u0, int u1, __amdgpu_buffer_rsrc_t rso, u32 total, int tid) {
#pragma unroll
    for (int u = u0; u < u1; ++u) {
        if (u < NOUT) {
            const u32 j = u * 1024 + tid;
            const u32 off = (u < CH / 2048 || j < (total >> 1)) ? j * 12 : 0x80000000u;
            __builtin_amdgcn_raw_buffer_store_b96(o.d[u], rso, off, 0, AUX_WT);
        }
    }
}

template <int V>
__global__ __launch_bounds__(1024) void p1_partition(const u32x4* __restrict__ ii, const u32x4* __restrict__ io,
                                                     const u32x2* __restrict__ an, const u16* __restrict__ ang_freq,
                                                     const u32x4* __restrict__ m4, char* __restrict__ rec,
                                                     u32* __restrict__ tab, u32* __restrict__ meta) {
    constexpr u32 H0 = L1_H, H1 = L1_H + 8192;
    const int tid = threadIdx.x;
    const int c0 = blockIdx.x, c1 = blockIdx.x + NWG1;
    const u32 fbits = ang_freq[0];
    __builtin_amdgcn_sched_barrier(0);
    P1In in;
    in.io = __builtin_amdgcn_make_buffer_rsrc((void*)io, 0, (u32)T_ * 4u, 0x00020000);
    in.ii = __builtin_amdgcn_make_buffer_rsrc((void*)ii, 0, (u32)T_ * 4u, 0x00020000);
    in.an = __builtin_amdgcn_make_buffer_rsrc((void*)an, 0, (u32)T_ * 2u, 0x00020000);
    Raw raw;
#pragma unroll
    for (int u = 0; u < 5; ++u) p1_load_group(raw, u, in, c0, tid);
    *(lds_u32x4*)(size_t)(L1_H + tid * 16) = (u32x4){0u, 0u, 0u, 0u};
    if (tid < 2) *L32(L1_W + 64 + tid * 4) = 0u;
    const Freq freq = make_freq(bf2f(fbits));
    __syncthreads();
    float nanacc = 0.0f;
    u32 lo[20], hi[20];
#pragma unroll
    for (int u = 0; u < 5; ++u) {
        if (u < 4 || tid < QLAST) p1_convert(raw, u, H0, freq, nanacc, lo + 4 * u, hi + 4 * u);
        p1_load_group(raw, u, in, c1, tid);
    }
    __syncthreads();
    Scan sc;
    p1_scan_a(sc, H0, tid);
    __syncthreads();
    const u32 total0 = p1_scan_b(sc, H0, tid);
    __syncthreads();
    p1_store_tab(tab, c0, tid);
#pragma unroll
    for (int u = 0; u < 5; ++u) {
        if (u < 4 || tid < QLAST) {
            u32 pos[4];
#pragma unroll
            for (int r = 0; r < 4; ++r) pos[r] = lds_add32_rtn(H0 + (hi[4 * u + r] >> 16), 1u);
            u32 lo1[4], hi1[4];
            p1_convert(raw, u, H1, freq, nanacc, lo1, hi1);
#pragma unroll
            for (int r = 0; r < 4; ++r) {
                *L32(L1_LO + pos[r] * 4) = lo[4 * u + r];
                *L16(L1_C + pos[r] * 2) = (u16)hi[4 * u + r];
                lo[4 * u + r] = lo1[r];
                hi[4 * u + r] = hi1[r];
            }
        }
        __builtin_amdgcn_sched_barrier(0);
    }
    constexpr int PER = E_ * 2 / NWG1;
    u32x4 mv[3];
    mv[0] = m4[blockIdx.x * PER + tid];
    mv[1] = m4[blockIdx.x * PER + 1024 + tid];
    mv[2] = m4[blockIdx.x * PER + min(2048 + tid, PER - 1)];
    __builtin_amdgcn_sched_barrier(0);
    __syncthreads();
    p1_scan_a(sc, H1, tid);
    Out o0;
    p1_out_read(o0, tid);
    const auto rs0 = __builtin_amdgcn_make_buffer_rsrc((void*)(rec + (size_t)c0 * (CHP2 * 12)), 0, (u32)CHP2 * 12u, 0x00020000);
    p1_out_store(o0, 0, 2, rs0, total0, tid);
    __builtin_amdgcn_sched_barrier(0);
    __syncthreads();
    const u32 total1 = p1_scan_b(sc, H1, tid);
    p1_out_store(o0, 2, 4, rs0, total0, tid);
    __builtin_amdgcn_sched_barrier(0);
    __syncthreads();
    p1_store_tab(tab, c1, tid);
#pragma unroll
    for (int u = 0; u < 5; ++u) {
        if (u < 4 || tid < QLAST) {
            u32 pos[4];
#pragma unroll
            for (int r = 0; r < 4; ++r) pos[r] = lds_add32_rtn(H1 + (hi[4 * u + r] >> 16), 1u);
#pragma unroll
            for (int r = 0; r < 4; ++r) {
                *L32(L1_LO + pos[r] * 4) = lo[4 * u + r];
                *L16(L1_C + pos[r] * 2) = (u16)hi[4 * u + r];
            }
        }
        p1_out_store(o0, 4 + (u * 7 + 2) / 5, 4 + ((u + 1) * 7 + 2) / 5, rs0, total0, tid);
        __builtin_amdgcn_sched_barrier(0);
    }
    {
        u32 mx = 0;
#pragma unroll
        for (int u = 0; u < 3; ++u)
#pragma unroll
            for (int k = 0; k < 4; ++k) mx = max(mx, max(mv[u][k] & 0x7fffu, (mv[u][k] >> 16) & 0x7fffu));
        mx = wave_scan_max(mx);
        if ((tid & 63) == 63) __hip_atomic_fetch_max(L32(L1_W + 64), mx, __ATOMIC_RELAXED, __HIP_MEMORY_SCOPE_WORKGROUP);
        if (nanacc != nanacc) *L32(L1_W + 68) = 1u;
    }
    __syncthreads();
    {
        Out o1;
        p1_out_read(o1, tid);
        const auto rs1 = __builtin_amdgcn_make_buffer_rsrc((void*)(rec + (size_t)c1 * (CHP2 * 12)), 0, (u32)CHP2 * 12u, 0x00020000);
        p1_out_store(o1, 0, NOUT, rs1, total1, tid);
    }
    if (tid == 0) meta[blockIdx.x] = *L32(L1_W + 64) | (*L32(L1_W + 68) << 16);
    if (blockIdx.x == 1 && tid < NNULL) {
        const u32 row = RB + ((tid >> 2) & 15);
        u32* p = (u32*)(rec + ((size_t)NULLP + tid) * 12);
        p[0] = row << 16; p[1] = 0u; p[2] = row;
    }
}


struct EpiW { s16x4 wd, wu, ws, wr; u32 cen; };

__device__ __forceinline__ u32 pack_bf(u32 a, u32 b) { return a | (b << 16); }

__device__ __forceinline__ void epi_weights(EpiW& w, const u16* __restrict__ w_rbf, const u16* __restrict__ w_sbf,
                                            const u16* __restrict__ w_down, const u16* __restrict__ w_up,
                                            const u16* __restrict__ centers, int lane) {
    const int fo = lane & 15, g = lane >> 4;
    w.wd = __builtin_bit_cast(s16x4, (u32x2){pack_bf(w_down[(4 * g + 0) * 16 + fo], w_down[(4 * g + 1) * 16 + fo]),
                                             pack_bf(w_down[(4 * g + 2) * 16 + fo], w_down[(4 * g + 3) * 16 + fo])});
    w.wu = __builtin_bit_cast(s16x4, (u32x2){pack_bf(w_up[(4 * g + 0) * 16 + fo], w_up[(4 * g + 1) * 16 + fo]),
                                             pack_bf(w_up[(4 * g + 2) * 16 + fo], w_up[(4 * g + 3) * 16 + fo])});
    const u32 s01 = pack_bf(w_sbf[(2 * g) * 16 + fo], w_sbf[(2 * g + 1) * 16 + fo]);
    const u32 r01 = pack_bf(w_rbf[(2 * g) * 16 + fo], w_rbf[(2 * g + 1) * 16 + fo]);
    w.ws = __builtin_bit_cast(s16x4, (u32x2){s01, s01});
    w.wr = __builtin_bit_cast(s16x4, (u32x2){r01, r01});
    w.cen = pack_bf(centers[2 * g], centers[2 * g + 1]);
}

__device__ __forceinline__ void epi_gates(const EpiW& w, u32 dbits, f32x4& sg, f32x4& gt) {
    const f32x4 z = {0.f, 0.f, 0.f, 0.f};
    const float KE = -14.426950408889634f;
    const float d = bf2f(dbits);
    const float x0 = d - __uint_as_float(w.cen << 16), x1 = d - __uint_as_float(w.cen & 0xffff0000u);
    const float r0 = __builtin_amdgcn_exp2f(x0 * KE * x0), r1 = __builtin_amdgcn_exp2f(x1 * KE * x1);
    const u32 rh = cvt_pk_bf16(r0, r1);
    const u32 rl = cvt_pk_bf16(r0 - __uint_as_float(rh << 16), r1 - __uint_as_float(rh & 0xffff0000u));
    const s16x4 rb = __builtin_bit_cast(s16x4, (u32x2){rh, rl});
    sg = __builtin_amdgcn_mfma_f32_16x16x16bf16_1k(w.ws, rb, z, 0, 0, 0);
    gt = __builtin_amdgcn_mfma_f32_16x16x16bf16_1k(w.wr, rb, z, 0, 0, 0);
}
__device__ __forceinline__ u32x2 epi_rows(const EpiW& w, float a0, float a1, float a2, float a3, float scale,
                                          const f32x4& sg, const f32x4& gt, u32x2 mv) {
    const f32x4 z = {0.f, 0.f, 0.f, 0.f};
    const s16x4 av = __builtin_bit_cast(s16x4, (u32x2){cvt_pk_bf16(a0, a1), cvt_pk_bf16(a2, a3)});
    const f32x4 md = __builtin_amdgcn_mfma_f32_16x16x16bf16_1k(w.wd, av, z, 0, 0, 0);
    const f32x4 ss = sg * scale;
    const s16x4 ag = __builtin_bit_cast(s16x4, (u32x2){cvt_pk_bf16(md[0] * ss[0], md[1] * ss[1]),
                                                       cvt_pk_bf16(md[2] * ss[2], md[3] * ss[3])});
    const f32x4 u = __builtin_amdgcn_mfma_f32_16x16x16bf16_1k(w.wu, ag, z, 0, 0, 0);
    const float o0 = __builtin_fmaf(u[0], gt[0], __uint_as_float(mv[0] << 16));
    const float o1 = __builtin_fmaf(u[1], gt[1], __uint_as_float(mv[0] & 0xffff0000u));
    const float o2 = __builtin_fmaf(u[2], gt[2], __uint_as_float(mv[1] << 16));
    const float o3 = __builtin_fmaf(u[3], gt[3], __uint_as_float(mv[1] & 0xffff0000u));
    return (u32x2){cvt_pk_bf16(o0, o1), cvt_pk_bf16(o2, o3)};
}

__device__ __forceinline__ void decode_pair(u32 alo, u32 ahi, u32 ckl, u32 ckh, float& f0, float& f1) {
    u32 borrow;
    const u32 slo = __builtin_subc(alo, ckl, 0u, &borrow);
    const u32 shi = ahi - ckh - borrow - ckl + (slo >> 31);
    const float fs = (float)(int)slo, fd = (float)(int)shi;
    f0 = fs + fd;
    f1 = fs - fd;
}


struct FE {
    u32 val, cumv;
    u32 n[S];
    u32 rounds[S];
    u32 s, r;
    u32 k00, cap;
    u32 carry, mark, tabl;
    u32 cbase;
};
struct RegSet { u32x3 d[NSB]; u32 nw, soff; bool live; };

__device__ __forceinline__ void fe_fetch(FE& f, RegSet& R, int lane, __amdgpu_buffer_rsrc_t rsr) {
    R.live = f.s < (u32)S;
    const u32 n0 = f.n[0];
    const u32 k00 = f.k00;
    R.nw = R.live ? min(n0 - min(n0, k00), f.cap) : 0u;
    R.soff = min(f.s, (u32)S - 1) * SBYTES;
    if (R.nw == 0) {
        R.d[0] = (u32x3){(u32)(RB + (lane >> 2)) << 16, 0u, (u32)(RB + (lane >> 2))};
        return;
    }
    if (f.r == 0) {
        const u32 t = *(volatile lds_u32*)(size_t)(f.tabl + f.s * 256);
        const u32 len = t >> 16, start = t & 0xffffu;
        const u32 cum = wave_scan_add(len) - len;
        f.val = (lane == 32 ? NULLP - n0 : f.cbase + start - cum) + (f.s << 24);
        f.cumv = lane == 32 ? n0 : (len ? cum : 0x7fffffffu);
    }
    const u32 slot = min(f.cumv - k00, (u32)CAPW + lane);
    *(volatile lds_u32*)(size_t)(f.mark + slot * 4) = f.val;
    u32 v[NSB];
#pragma unroll
    for (int j = 0; j < NSB; ++j) v[j] = *(volatile lds_u32*)(size_t)(f.mark + (j * 64 + lane) * 4);
#pragma unroll
    for (int j = 0; j < NSB; ++j) v[j] = wave_scan_max(v[j]);
    u32 run = f.carry;
#pragma unroll
    for (int j = 0; j < NSB; ++j) {
        v[j] = max(v[j], run);
        run = __builtin_amdgcn_readlane(v[j], 63);
        if ((u32)(j * 64 + 64) <= f.cap) f.carry = run;
    }
#pragma unroll
    for (int j = 0; j < NSB; ++j) {
        if (j == 0 || (u32)(j * 64) < R.nw) {
            const u32 idx = (v[j] & 0xffffffu) + k00 + j * 64 + lane;
            R.d[j] = __builtin_amdgcn_raw_buffer_load_b96(rsr, __umul24(idx, 12u), 0, 0);
        }
    }
}
__device__ __forceinline__ void fe_advance(FE& f) {
    if (f.s >= (u32)S) return;
    f.r += 1;
    f.k00 += f.cap;
    f.cap = CAPW;
    if (f.r >= f.rounds[0]) {
#pragma unroll
        for (int s = 0; s + 1 < S; ++s) { f.n[s] = f.n[s + 1]; f.rounds[s] = f.rounds[s + 1]; }
        f.r = 0;
        f.k00 = 0;
        f.s += 1;
    }
}

struct Gat { u32x2 x0, x1, x2, x3; u32 la, b2; };
struct P2C { u32 q8, q16, psc2; __amdgpu_buffer_rsrc_t rsm; };

__device__ __forceinline__ Gat gat_issue(u32 lo, u32 b2, u32 soff, const P2C& k) {
    Gat g;
    const u32 ga = (lo & 0xffffu) << 5, row = lo >> 16;
    g.x0 = __builtin_amdgcn_raw_buffer_load_b64(k.rsm, quad_bcast<0>(ga) + k.q8, soff, 0);
    g.x1 = __builtin_amdgcn_raw_buffer_load_b64(k.rsm, quad_bcast<1>(ga) + k.q8, soff, 0);
    g.x2 = __builtin_amdgcn_raw_buffer_load_b64(k.rsm, quad_bcast<2>(ga) + k.q8, soff, 0);
    g.x3 = __builtin_amdgcn_raw_buffer_load_b64(k.rsm, quad_bcast<3>(ga) + k.q8, soff, 0);
    g.la = (row << 6) | ((row << 2) & 8u);
    g.b2 = b2;
    lds_add32(L2_CNT + row * 4, 1u);
    asm volatile("" : "+v"(g.la), "+v"(g.b2));
    __builtin_amdgcn_sched_barrier(0);
    return g;
}

__device__ __forceinline__ void gat_consume(const Gat& g, u32 q16) {
    auto step = [&](u32x2 x, u32 laj, u32 bj) {
        float s0, d0, s1, d1;
        asm("v_dot2_f32_bf16 %0, %1, %2, %3" : "=v"(s0) : "v"(x[0]), "v"(bj), "s"(KMAGIC));
        asm("v_dot2_f32_bf16 %0, %1, %2, %3 neg_hi:[0,1,0]" : "=v"(d0) : "v"(x[0]), "v"(bj), "s"(KMAGIC));
        asm("v_dot2_f32_bf16 %0, %1, %2, %3" : "=v"(s1) : "v"(x[1]), "v"(bj), "s"(KMAGIC));
        asm("v_dot2_f32_bf16 %0, %1, %2, %3 neg_hi:[0,1,0]" : "=v"(d1) : "v"(x[1]), "v"(bj), "s"(KMAGIC));
        const u32 a0 = laj + q16;
        lds_add64(a0, ((u64)__float_as_uint(d0) << 32) | __float_as_uint(s0));
        lds_add64(a0 ^ 8u, ((u64)__float_as_uint(d1) << 32) | __float_as_uint(s1));
    };
    step(g.x0, quad_bcast<0>(g.la), quad_bcast<0>(g.b2));
    step(g.x1, quad_bcast<1>(g.la), quad_bcast<1>(g.b2));
    step(g.x2, quad_bcast<2>(g.la), quad_bcast<2>(g.b2));
    step(g.x3, quad_bcast<3>(g.la), quad_bcast<3>(g.b2));
    __builtin_amdgcn_sched_barrier(0);
}

struct Half { u32 loa, lob, b2a, b2b; };
template <int J>
__device__ __forceinline__ Half sb_decode(const RegSet& R, const P2C& k) {
    u32x3 d = R.d[J];
    asm volatile("" : "+v"(d));
    const u32 cp = (d[1] & 0xffffu) | (d[2] & 0xffff0000u);
    const u32 cs = __builtin_bit_cast(u32, __builtin_bit_cast(u16x2, cp) + __builtin_bit_cast(u16x2, k.psc2));
    Half h;
    h.loa = d[0];
    h.lob = __builtin_amdgcn_alignbit(d[2], d[1], 16);
    h.b2a = __builtin_amdgcn_perm(cs, cs, 0x01000100u);
    h.b2b = __builtin_amdgcn_perm(cs, cs, 0x03020302u);
    return h;
}

template <int J>
__device__ __forceinline__ void sb_step(const RegSet& R, u32 nsb, const Half& h, const Gat& g0, const P2C& k) {
    const Gat g1 = gat_issue(h.lob, h.b2b, R.soff, k);
    gat_consume(g0, k.q16);
    if constexpr (J + 1 < NSB) {
        if (nsb > (u32)(J + 1)) {
            const Half hn = sb_decode<J + 1>(R, k);
            if (R.nw <= (u32)((J + 1) * 64 + 32)) {
                const auto ml = __builtin_amdgcn_permlane32_swap(hn.loa, hn.lob, false, false);
                const auto mb = __builtin_amdgcn_permlane32_swap(hn.b2a, hn.b2b, false, false);
                const Gat gn = gat_issue(ml[0], mb[0], R.soff, k);
                gat_consume(g1, k.q16);
                gat_consume(gn, k.q16);
                return;
            }
            const Gat gn = gat_issue(hn.loa, hn.b2a, R.soff, k);
            gat_consume(g1, k.q16);
            sb_step<J + 1>(R, nsb, hn, gn, k);
            return;
        }
    }
    gat_consume(g1, k.q16);
}

template <int V>
__global__ __launch_bounds__(1024) void p2_accumulate(const char* __restrict__ rec, const u32* __restrict__ tab,
                                                      const u32* __restrict__ meta, const u16* __restrict__ m,
                                                      const u16* __restrict__ dist, const u16* __restrict__ centers,
                                                      const u16* __restrict__ w_rbf, const u16* __restrict__ w_sbf,
                                                      const u16* __restrict__ w_down, const u16* __restrict__ w_up,
                                                      u16* __restrict__ out) {
    const int tid = threadIdx.x, lane = tid & 63;
    const int wave = __builtin_amdgcn_readfirstlane(tid >> 6);
    const int b = (blockIdx.x & 7) * 32 + (blockIdx.x >> 3);
    const int row0 = b * RB;
    const auto rsr = __builtin_amdgcn_make_buffer_rsrc((void*)rec, 0, (u32)(WS_TAB), 0x00020000);
    const auto rsm = __builtin_amdgcn_make_buffer_rsrc((void*)m, 0, (u32)E_ * 32u, 0x00020000);
    const auto rst = __builtin_amdgcn_make_buffer_rsrc((void*)tab, 0, (u32)NC * NB * S * 4u, 0x00020000);
    const auto rsq = __builtin_amdgcn_make_buffer_rsrc((void*)meta, 0, 1024u, 0x00020000);
    const int c = (lane & 31) * 16 + wave;
    const u32 toff = lane < 32 ? (u32)(c * NB + b) * (S * 4) : 0x80000000u;
    u32 t[S];
    {
        const u32x4 t0 = __builtin_amdgcn_raw_buffer_load_b128(rst, toff, 0, 0);
        t[0] = t0[0]; t[1] = t0[1]; t[2] = t0[2]; t[3] = t0[3];
        t[4] = __builtin_amdgcn_raw_buffer_load_b32(rst, toff + 16, 0, 0);
    }
    const u32x4 mt4 = __builtin_amdgcn_raw_buffer_load_b128(rsq, lane * 16, 0, 0);
    __builtin_amdgcn_sched_barrier(0);
    const u32 mark = L2_MARK + wave * ((CAPW + 64) * 4);
    *(lds_u32x4*)(size_t)(mark + lane * 16) = (u32x4){0u, 0u, 0u, 0u};
    if (lane < 32) *(lds_u32x4*)(size_t)(mark + 1024 + lane * 16) = (u32x4){0u, 0u, 0u, 0u};
    static_assert((CAPW + 64) * 4 == 1536, "marker area");
    for (u32 a = tid * 16; a < L2_MARK; a += 1024 * 16) *(lds_u32x4*)(size_t)a = (u32x4){0u, 0u, 0u, 0u};
    if (tid == 0) *L32(L2_FLAG) = 0u;
    const u32 mtm = max(max(mt4[0] & 0xffffu, mt4[1] & 0xffffu), max(mt4[2] & 0xffffu, mt4[3] & 0xffffu));
    const u32 mmax = __builtin_amdgcn_readlane(wave_scan_max(mtm), 63);
    const bool anynan = __builtin_amdgcn_ballot_w64(((mt4[0] | mt4[1] | mt4[2] | mt4[3]) & 0x10000u) != 0) != 0;
    const u32 eb = mmax >> 7;
    const bool fast = !anynan && (mmax == 0 || (eb >= 64 && eb <= 147));
    const u32 ebc = mmax == 0 ? 127u : min(max(eb, 64u), 147u);
    const u32 pexp = 147u - ebc;
    const float inv2 = __uint_as_float((126u - pexp) << 23);
    const int e = lane & 15, g = lane >> 4;
    EpiW w;
    bool slow = !fast;
    if (fast) {
        FE f;
        f.tabl = L2_TABW + wave * (S * 256) + lane * 4;
        f.cbase = (u32)c * CHP2;
#pragma unroll
        for (int s = 0; s < S; ++s) {
            f.n[s] = __builtin_amdgcn_readlane(wave_scan_add(t[s] >> 16), 63);
            *L32(f.tabl + s * 256) = t[s];
        }
        f.s = 0; f.r = 0; f.carry = 0; f.mark = mark;
        f.k00 = 0; f.cap = CAP0;
        P2C k;
        k.q8 = (lane & 3) * 8; k.q16 = (lane & 3) * 16 + L2_ACC;
        k.psc2 = (pexp << 7) * 0x10001u; k.rsm = rsm;
        RegSet cur, nxt;
        fe_fetch(f, nxt, lane, rsr);
        if (lane < S) {
            u32 v = f.n[0];
#pragma unroll
            for (int s = 1; s < S; ++s) v = lane == s ? f.n[s] : v;
            *L32(L2_NW + lane * 64 + wave * 4) = v;
        }
        cur = nxt;
#pragma unroll
        for (int j = 0; j < NSB; ++j) asm volatile("" : "+v"(cur.d[j]));
        __syncthreads();
#pragma unroll
        for (int s = 0; s < S; ++s) {
            const u32 v = *L32(L2_NW + s * 64 + (lane & 15) * 4);
            const u32 mx = __builtin_amdgcn_readlane(wave_scan_max(v), 63);
            f.rounds[s] = s == 0 ? 1u + (mx - min(mx, (u32)CAP0) + CAPW - 1) / CAPW : max((mx + CAPW - 1) / CAPW, 1u);
        }
        fe_advance(f);
        Half h = sb_decode<0>(cur, k);
        Gat g0 = gat_issue(h.loa, h.b2a, cur.soff, k);
        fe_fetch(f, nxt, lane, rsr);
        fe_advance(f);
        if (wave == 0) {
            epi_weights(w, w_rbf, w_sbf, w_down, w_up, centers, lane);
            *(lds_u32x2*)(size_t)(L2_WD + lane * 8) = __builtin_bit_cast(u32x2, w.wd);
            *(lds_u32x2*)(size_t)(L2_WU + lane * 8) = __builtin_bit_cast(u32x2, w.wu);
            *(lds_u32x2*)(size_t)(L2_WS + lane * 8) = __builtin_bit_cast(u32x2, w.ws);
            *(lds_u32x2*)(size_t)(L2_WR + lane * 8) = __builtin_bit_cast(u32x2, w.wr);
            *L32(L2_CEN + lane * 4) = w.cen;
        } else if (wave < 4) {
#pragma unroll
            for (int u = 0; u < 4; ++u) {
                const int r = ((wave - 1) * 4 + u) * 128 + lane * 2;
                if (r < RB) *L32(L2_DIST + r * 2) = *(const u32*)(dist + row0 + r);
            }
        }
#pragma clang loop unroll(disable)
        while (cur.live) {
            sb_step<0>(cur, (cur.nw + 63) >> 6, h, g0, k);
            cur = nxt;
#pragma unroll
            for (int j = 0; j < NSB; ++j) asm volatile("" : "+v"(cur.d[j]));
            h = sb_decode<0>(cur, k);
            g0 = gat_issue(h.loa, h.b2a, cur.soff, k);
            fe_fetch(f, nxt, lane, rsr);
            fe_advance(f);
            __syncthreads();
        }
        asm volatile("" :: "v"(g0.x0), "v"(g0.x1), "v"(g0.x2), "v"(g0.x3));
        const auto rso = __builtin_amdgcn_make_buffer_rsrc((void*)(out + (size_t)row0 * 16), 0, (u32)RB * 32u, 0x00020000);
        const auto rsi = __builtin_amdgcn_make_buffer_rsrc((void*)(m + (size_t)row0 * 16), 0, (u32)RB * 32u, 0x00020000);
        u32x2 mv[5];
        f32x4 sg[5], gt[5];
#pragma unroll
        for (int u = 0; u < 5; ++u) mv[u] = __builtin_amdgcn_raw_buffer_load_b64(rsi, ((wave + 16 * u) * 16 + e) * 32 + g * 8, 0, 0);
        w.wd = __builtin_bit_cast(s16x4, *(lds_u32x2*)(size_t)(L2_WD + lane * 8));
        w.wu = __builtin_bit_cast(s16x4, *(lds_u32x2*)(size_t)(L2_WU + lane * 8));
        w.ws = __builtin_bit_cast(s16x4, *(lds_u32x2*)(size_t)(L2_WS + lane * 8));
        w.wr = __builtin_bit_cast(s16x4, *(lds_u32x2*)(size_t)(L2_WR + lane * 8));
        w.cen = *L32(L2_CEN + lane * 4);
#pragma unroll
        for (int u = 0; u < 5; ++u) {
            const int rc = min((wave + 16 * u) * 16 + e, RB - 1);
            epi_gates(w, *L16(L2_DIST + rc * 2), sg[u], gt[u]);
        }
        u32 over = 0;
#pragma unroll
        for (int u = 0; u < 5; ++u) {
            const int r = (wave + 16 * u) * 16 + e;
            const int rc = min(r, RB - 1);
            const u32 aa = L2_ACC + rc * 64 + g * 16 + ((rc << 2) & 8);
            const u32x2 p0 = *(lds_u32x2*)(size_t)aa, p1 = *(lds_u32x2*)(size_t)(aa ^ 8);
            const u32 cn = *L32(L2_CNT + rc * 4);
            over = max(over, cn);
            const u32 q = __umul24(cn, KBITS >> 22);
            const u32 ckl = q << 22, ckh = q >> 10;
            float a0, a1, a2, a3;
            decode_pair(p0[0], p0[1], ckl, ckh, a0, a1);
            decode_pair(p1[0], p1[1], ckl, ckh, a2, a3);
            const u32x2 o = epi_rows(w, a0, a1, a2, a3, inv2, sg[u], gt[u], mv[u]);
            __builtin_amdgcn_raw_buffer_store_b64(o, rso, r * 32 + g * 8, 0, AUX_WT);
        }
        if (over > 511u) *L32(L2_FLAG) = 1u;
        asm volatile("s_waitcnt vmcnt(0)" ::: "memory");
        __syncthreads();
        slow = *L32(L2_FLAG) != 0;
    }
    if (!slow) return;
    const u32x2* m2 = (const u32x2*)m;
    u32x2* out2 = (u32x2*)out;
    epi_weights(w, w_rbf, w_sbf, w_down, w_up, centers, lane);
    for (int half = 0; half < 2; ++half) {
        __syncthreads();
        for (u32 a = tid * 16; a < 625 * 128; a += 1024 * 16) *(lds_u32x4*)(size_t)a = (u32x4){0u, 0u, 0u, 0u};
        __syncthreads();
        for (int s = 0; s < S; ++s) {
            for (int i = 0; i < 32; ++i) {
                const int cc = i * 16 + wave;
                const u32 tt = tab[((size_t)cc * NB + b) * S + s];
                const u32 len = (tt >> 16) * 2, start = (tt & 0xffffu) * 2;
                for (u32 j0 = 0; j0 < len; j0 += 4) {
                    const u32 j = j0 + (lane >> 4);
                    if (j < len) {
                        const u16* rp = (const u16*)(rec + ((size_t)cc * CHP2 * 2 + start + j) * 6);
                        const u32 il = rp[0], ol = rp[1], cb = rp[2];
                        const int r = (int)ol - half * 625;
                        if (r >= 0 && r < 625) {
                            const float x = bf2f(m[((size_t)s * SR + il) * 16 + (lane & 15)]);
                            __hip_atomic_fetch_add((lds_f64*)(size_t)(r * 128 + (lane & 15) * 8), (double)(bf2f(cb) * x),
                                                   __ATOMIC_RELAXED, __HIP_MEMORY_SCOPE_WORKGROUP);
                        }
                    }
                }
            }
        }
        __syncthreads();
        for (int gi = wave; gi < 40; gi += 16) {
            const int r = gi * 16 + e;
            const bool valid = r < 625;
            const int rc = valid ? r : 624;
            const int grow = row0 + half * 625 + rc;
            const lds_f64* p = (const lds_f64*)(size_t)(rc * 128 + g * 32);
            f32x4 sg, gt;
            epi_gates(w, dist[grow], sg, gt);
            const u32x2 o = epi_rows(w, (float)p[0], (float)p[1], (float)p[2], (float)p[3], 1.0f, sg, gt, m2[(size_t)grow * 4 + g]);
            if (valid) out2[(size_t)grow * 4 + g] = o;
        }
    }
}

}

extern "C" void kernel_launch(void* const* d_in, const int* in_sizes, int n_in, void* d_out, int out_size, void* d_ws,
                              size_t ws_size, hipStream_t stream) {
    const u16* m = (const u16*)d_in[0];
    const int* idx_in = (const int*)d_in[1];
    const int* idx_out = (const int*)d_in[2];
    const u16* dist = (const u16*)d_in[3];
    const u16* angle = (const u16*)d_in[4];
    const u16* centers = (const u16*)d_in[5];
    const u16* ang_freq = (const u16*)d_in[6];
    const u16* w_rbf = (const u16*)d_in[7];
    const u16* w_sbf = (const u16*)d_in[8];
    const u16* w_down = (const u16*)d_in[9];
    const u16* w_up = (const u16*)d_in[10];
    char* ws = (char*)d_ws;
    (void)hipFuncSetAttribute((const void*)p1_partition<0>, hipFuncAttributeMaxDynamicSharedMemorySize, L1_SIZE);
    (void)hipFuncSetAttribute((const void*)p2_accumulate<0>, hipFuncAttributeMaxDynamicSharedMemorySize, L2_SIZE);
    p1_partition<0><<<NWG1, 1024, L1_SIZE, stream>>>((const u32x4*)idx_in, (const u32x4*)idx_out, (const u32x2*)angle, ang_freq,
                                                  (const u32x4*)m, ws + WS_REC, (u32*)(ws + WS_TAB), (u32*)(ws + WS_META));
    p2_accumulate<0><<<NB, 1024, L2_SIZE, stream>>>(ws + WS_REC, (const u32*)(ws + WS_TAB), (const u32*)(ws + WS_META), m, dist,
                                                 centers, w_rbf, w_sbf, w_down, w_up, (u16*)d_out);
}
```

```cpp
#include <hip/hip_runtime.h>
#include <hip/hip_bf16.h>
#include <stdint.h>

typedef unsigned int u32;
typedef unsigned long long u64;
typedef unsigned short u16;
using u16x2 = __attribute__((ext_vector_type(2))) unsigned short;
using u32x2 = __attribute__((ext_vector_type(2))) unsigned;
using u32x3 = __attribute__((ext_vector_type(3))) unsigned;
using u32x4 = __attribute__((ext_vector_type(4))) unsigned;
using f32x2 = __attribute__((ext_vector_type(2))) float;
using f32x4 = __attribute__((ext_vector_type(4))) float;
using bf16x2 = __attribute__((ext_vector_type(2))) __bf16;
using s16x4 = __attribute__((ext_vector_type(4))) short;
typedef __attribute__((address_space(3))) u64 lds_u64;
typedef __attribute__((address_space(3))) u32 lds_u32;
typedef __attribute__((address_space(3))) u16 lds_u16;
typedef __attribute__((address_space(3))) double lds_f64;
typedef __attribute__((address_space(3))) u32x2 lds_u32x2;
typedef __attribute__((address_space(3))) u32x4 lds_u32x4;

namespace {

constexpr int E_ = 320000;
constexpr int T_ = 10240000;
constexpr int NB = 256;
constexpr int RB = 1250;
constexpr int S = 5;
constexpr int SR = 65536;
constexpr u32 SBYTES = (u32)SR * 32u;
constexpr int CH = 20000;
constexpr int QLAST = CH / 4 - 4096;
constexpr int CHP2 = (CH + NB * S) / 2;
constexpr int NC = 512;
constexpr int NWG1 = 256;
constexpr u32 MAGIC_RB = 3435974u;
static_assert(NC * CH == T_ && NC == 2 * NWG1 && NC == 512 && NWG1 == 256 && NB * RB == E_ && S * SR >= E_, "shapes");

constexpr u32 NULLP = (u32)NC * CHP2;
constexpr int NNULL = 512;
constexpr size_t WS_REC = 0;
constexpr size_t WS_TAB = ((size_t)NULLP + NNULL) * 12 + 2048;
constexpr size_t WS_META = WS_TAB + (size_t)NC * NB * S * 4;
static_assert(WS_TAB % 128 == 0 && WS_META % 128 == 0, "alignment");

constexpr u32 L1_LO = 0;
constexpr u32 L1_C = CHP2 * 8;
constexpr u32 L1_H = CHP2 * 12;
constexpr u32 L1_TAB = L1_H + 16384;
constexpr u32 L1_W = L1_TAB + NB * S * 4;
constexpr u32 L1_SIZE = L1_W + 128;

constexpr int NSB = 5;
constexpr int CAPW = NSB * 64;
constexpr int CAP0 = 128;
constexpr u32 L2_CNT = 0;
constexpr u32 L2_ACC = 5120;
constexpr u32 L2_MARK = L2_ACC + (RB + 16) * 64;
constexpr u32 L2_FLAG = L2_MARK + 16 * (CAPW + 64) * 4;
constexpr u32 L2_NW = L2_FLAG + 64;
constexpr u32 L2_TABW = L2_NW + 8 * 64;
constexpr u32 L2_WD = L2_TABW + 16 * S * 256;
constexpr u32 L2_WU = L2_WD + 512;
constexpr u32 L2_WS = L2_WU + 512;
constexpr u32 L2_WR = L2_WS + 512;
constexpr u32 L2_CEN = L2_WR + 512;
constexpr u32 L2_DIST = L2_CEN + 256;
constexpr u32 L2_SIZE = L2_DIST + 2560;
static_assert(NNULL >= CAPW + 64 && NULLP + NNULL < (1u << 24), "null records");
static_assert(L2_SIZE <= 163840 && L1_SIZE <= 163840 && L2_SIZE % 16 == 0, "LDS");

constexpr int AUX_NT = 2;
constexpr int AUX_WT = 16;

constexpr float KMAGIC = 12582912.0f;
constexpr u32 KBITS = 0x4B400000u;

__device__ __forceinline__ lds_u32* L32(u32 a) { return (lds_u32*)(size_t)a; }
__device__ __forceinline__ lds_u64* L64(u32 a) { return (lds_u64*)(size_t)a; }
__device__ __forceinline__ lds_u16* L16(u32 a) { return (lds_u16*)(size_t)a; }
__device__ __forceinline__ void lds_add32(u32 a, u32 v) { __hip_atomic_fetch_add(L32(a), v, __ATOMIC_RELAXED, __HIP_MEMORY_SCOPE_WORKGROUP); }
__device__ __forceinline__ u32 lds_add32_rtn(u32 a, u32 v) { return __hip_atomic_fetch_add(L32(a), v, __ATOMIC_RELAXED, __HIP_MEMORY_SCOPE_WORKGROUP); }
__device__ __forceinline__ void lds_add64(u32 a, u64 v) { __hip_atomic_fetch_add(L64(a), v, __ATOMIC_RELAXED, __HIP_MEMORY_SCOPE_WORKGROUP); }
__device__ __forceinline__ float bf2f(u32 v) { return __uint_as_float(v << 16); }

template <int CTRL, int RM = 0xf, bool BC = true>
__device__ __forceinline__ u32 dpp0(u32 v) { return (u32)__builtin_amdgcn_update_dpp(0, (int)v, CTRL, RM, 0xf, BC); }
template <int J>
__device__ __forceinline__ u32 quad_bcast(u32 v) { return (u32)__builtin_amdgcn_mov_dpp((int)v, J * 0x55, 0xf, 0xf, true); }

__device__ __forceinline__ u32 wave_scan_add(u32 v) {
    v += dpp0<0x111>(v); v += dpp0<0x112>(v); v += dpp0<0x114>(v); v += dpp0<0x118>(v);
    v += dpp0<0x142, 0xa, false>(v); v += dpp0<0x143, 0xc, false>(v);
    return v;
}
__device__ __forceinline__ u32 wave_scan_max(u32 v) {
    v = max(v, dpp0<0x111>(v)); v = max(v, dpp0<0x112>(v)); v = max(v, dpp0<0x114>(v)); v = max(v, dpp0<0x118>(v));
    v = max(v, dpp0<0x142, 0xa, false>(v)); v = max(v, dpp0<0x143, 0xc, false>(v));
    return v;
}
__device__ __forceinline__ u32 cvt_pk_bf16(float lo, float hi) {
    return __builtin_bit_cast(u32, __builtin_convertvector((f32x2){lo, hi}, bf16x2));
}

struct Freq { float hi, lo; };
__device__ __forceinline__ Freq make_freq(float f) {
    const float C_HI = 0.15915493667125702f;
    const float C_LO = 6.4206382985757765e-09f;
    Freq r;
    r.hi = f * C_HI;
    r.lo = __builtin_fmaf(f, C_LO, __builtin_fmaf(f, C_HI, -r.hi));
    return r;
}
__device__ __forceinline__ float cos_rev(float a, const Freq& f) {
    const float n = __builtin_rintf(a * f.hi);
    float r = __builtin_fmaf(a, f.hi, -n);
    r = __builtin_fmaf(a, f.lo, r);
    return __builtin_amdgcn_cosf(r);
}


struct Raw { u32x4 o[5]; u32x4 i[5]; u32x2 a[5]; };
struct P1In { __amdgpu_buffer_rsrc_t io, ii, an; };

__device__ __forceinline__ void p1_load_group(Raw& r, int u, const P1In& in, int half, int wg, int tid) {
    u32 q = half * (T_ / 8) + u * (NWG1 * 1024) + wg * (u < 4 ? 1024 : QLAST) + tid;
    if (u == 4 && tid >= QLAST) q = 0x0ffffff0u;
    r.o[u] = __builtin_amdgcn_raw_buffer_load_b128(in.io, q * 16, 0, AUX_NT);
    r.i[u] = __builtin_amdgcn_raw_buffer_load_b128(in.ii, q * 16, 0, AUX_NT);
    r.a[u] = __builtin_amdgcn_raw_buffer_load_b64(in.an, q * 8, 0, AUX_NT);
    __builtin_amdgcn_sched_barrier(0);
}

__device__ __forceinline__ void p1_convert(const Raw& raw, int u, u32 H, const Freq& freq, float& nanacc, u32* lo, u32* hi) {
#pragma unroll
    for (int j = 0; j < 4; ++j) {
        const u32 o = raw.o[u][j], i = raw.i[u][j];
        const u32 aw = raw.a[u][j >> 1];
        const float a = (j & 1) ? __uint_as_float(aw & 0xffff0000u) : __uint_as_float(aw << 16);
        const float c = cos_rev(a, freq);
        nanacc = __builtin_fmaf(c, 0.0f, nanacc);
        const u32 b = (u32)(((u64)(o & 0xffffffu) * MAGIC_RB) >> 32);
        u32 ol;
        asm("v_mad_i32_i24 %0, %1, %2, %3" : "=v"(ol) : "v"(b), "s"(-RB), "v"(o));
        lo[j] = __builtin_amdgcn_perm(ol, i, 0x05040100u);
        hi[j] = ((((i >> 8) & 0xff00u) | b) << 18) | cvt_pk_bf16(c, 0.0f);
        lds_add32(H + (hi[j] >> 16), 1u);
    }
#pragma unroll
    for (int j = 0; j < 4; ++j) asm volatile("" : "+v"(hi[j]), "+v"(lo[j]));
    __builtin_amdgcn_sched_barrier(0);
}

struct Scan { u32x2 cnt; u32 p0, p1, sum, incl; };
__device__ __forceinline__ void p1_scan_a(Scan& sc, u32 H, int tid) {
    sc.cnt = *(lds_u32x2*)(size_t)(H + tid * 8);
    sc.p0 = (sc.cnt[0] + 1) & ~1u; sc.p1 = (sc.cnt[1] + 1) & ~1u;
    sc.sum = sc.p0 + sc.p1;
    sc.incl = wave_scan_add(sc.sum);
    if ((tid & 63) == 63) *L32(L1_W + (tid >> 6) * 4) = sc.incl;
}
__device__ __forceinline__ u32 p1_scan_b(const Scan& sc, u32 H, int tid) {
    const int lane = tid & 63;
    const u32 wraw = *L32(L1_W + (lane & 15) * 4);
    const u32 wsum = wave_scan_add(lane < 16 ? wraw : 0u);
    const u32 total = __builtin_amdgcn_readlane(wsum, 15);
    const u32 woff = __builtin_amdgcn_readlane(wsum - wraw, __builtin_amdgcn_readfirstlane(tid >> 6));
    const u32 st0 = woff + sc.incl - sc.sum, st1 = st0 + sc.p0;
    *(lds_u32x2*)(size_t)(H + tid * 8) = (u32x2){st0, st1};
    if (tid < NB * S / 2) {
        const int s = (2 * tid) >> 8, b = (2 * tid) & 255;
        *L32(L1_TAB + (b * S + s) * 4) = (st0 >> 1) | (sc.p0 << 15);
        *L32(L1_TAB + ((b + 1) * S + s) * 4) = (st1 >> 1) | (sc.p1 << 15);
        if (sc.cnt[0] & 1) { *L32(L1_LO + (st0 + sc.cnt[0]) * 4) = (u32)RB << 16; *L16(L1_C + (st0 + sc.cnt[0]) * 2) = 0; }
        if (sc.cnt[1] & 1) { *L32(L1_LO + (st1 + sc.cnt[1]) * 4) = (u32)RB << 16; *L16(L1_C + (st1 + sc.cnt[1]) * 2) = 0; }
    }
    return total;
}
__device__ __forceinline__ void p1_store_tab(u32* __restrict__ tab, int chunk, int tid) {
    if (tid < NB * S / 4) {
        const auto rtb = __builtin_amdgcn_make_buffer_rsrc((void*)(tab + (size_t)chunk * NB * S), 0, (u32)NB * S * 4u, 0x00020000);
        __builtin_amdgcn_raw_buffer_store_b128(*(lds_u32x4*)(size_t)(L1_TAB + tid * 16), rtb, tid * 16, 0, AUX_WT);
    }
}
constexpr int NOUT = (CHP2 + 1023) / 1024;
struct Out { u32x3 d[NOUT]; };
__device__ __forceinline__ void p1_out_read(Out& o, int tid) {
#pragma unroll
    for (int u = 0; u < NOUT; ++u) {
        const int j = u * 1024 + tid;
        const u32x2 l = *(lds_u32x2*)(size_t)(L1_LO + j * 8);
        const u32 cc = *L32(L1_C + j * 4);
        o.d[u] = (u32x3){l[0], __builtin_amdgcn_perm(l[1], cc, 0x05040100u), __builtin_amdgcn_perm(cc, l[1], 0x07060302u)};
    }
}
__device__ __forceinline__ void p1_out_store(const Out& o, int u0, int u1, __amdgpu_buffer_rsrc_t rso, u32 total, int tid) {
#pragma unroll
    for (int u = u0; u < u1; ++u) {
        if (u < NOUT) {
            const u32 j = u * 1024 + tid;
            const u32 off = (u < CH / 2048 || j < (total >> 1)) ? j * 12 : 0x80000000u;
            __builtin_amdgcn_raw_buffer_store_b96(o.d[u], rso, off, 0, AUX_WT);
        }
    }
}

__global__ __launch_bounds__(1024) void p1_partition(const u32x4* __restrict__ ii, const u32x4* __restrict__ io,
                                                     const u32x2* __restrict__ an, const u16* __restrict__ ang_freq,
                                                     const u32x4* __restrict__ m4, char* __restrict__ rec,
                                                     u32* __restrict__ tab, u32* __restrict__ meta) {
    constexpr u32 H0 = L1_H, H1 = L1_H + 8192;
    const int tid = threadIdx.x;
    const int c0 = blockIdx.x, c1 = blockIdx.x + NWG1;
    const u32 fbits = ang_freq[0];
    __builtin_amdgcn_sched_barrier(0);
    P1In in;
    in.io = __builtin_amdgcn_make_buffer_rsrc((void*)io, 0, (u32)T_ * 4u, 0x00020000);
    in.ii = __builtin_amdgcn_make_buffer_rsrc((void*)ii, 0, (u32)T_ * 4u, 0x00020000);
    in.an = __builtin_amdgcn_make_buffer_rsrc((void*)an, 0, (u32)T_ * 2u, 0x00020000);
    Raw raw;
#pragma unroll
    for (int u = 0; u < 5; ++u) p1_load_group(raw, u, in, 0, blockIdx.x, tid);
    *(lds_u32x4*)(size_t)(L1_H + tid * 16) = (u32x4){0u, 0u, 0u, 0u};
    if (tid < 2) *L32(L1_W + 64 + tid * 4) = 0u;
    const Freq freq = make_freq(bf2f(fbits));
    __syncthreads();
    float nanacc = 0.0f;
    u32 lo[20], hi[20];
#pragma unroll
    for (int u = 0; u < 5; ++u) {
        if (u < 4 || tid < QLAST) p1_convert(raw, u, H0, freq, nanacc, lo + 4 * u, hi + 4 * u);
        p1_load_group(raw, u, in, 1, blockIdx.x, tid);
    }
    __syncthreads();
    Scan sc;
    p1_scan_a(sc, H0, tid);
    __syncthreads();
    const u32 total0 = p1_scan_b(sc, H0, tid);
    __syncthreads();
    p1_store_tab(tab, c0, tid);
#pragma unroll
    for (int u = 0; u < 5; ++u) {
        if (u < 4 || tid < QLAST) {
            u32 pos[4];
#pragma unroll
            for (int r = 0; r < 4; ++r) pos[r] = lds_add32_rtn(H0 + (hi[4 * u + r] >> 16), 1u);
            u32 lo1[4], hi1[4];
            p1_convert(raw, u, H1, freq, nanacc, lo1, hi1);
#pragma unroll
            for (int r = 0; r < 4; ++r) {
                *L32(L1_LO + pos[r] * 4) = lo[4 * u + r];
                *L16(L1_C + pos[r] * 2) = (u16)hi[4 * u + r];
                lo[4 * u + r] = lo1[r];
                hi[4 * u + r] = hi1[r];
            }
        }
        __builtin_amdgcn_sched_barrier(0);
    }
    constexpr int PER = E_ * 2 / NWG1;
    u32x4 mv[3];
    mv[0] = m4[blockIdx.x * PER + tid];
    mv[1] = m4[blockIdx.x * PER + 1024 + tid];
    mv[2] = m4[blockIdx.x * PER + min(2048 + tid, PER - 1)];
    __builtin_amdgcn_sched_barrier(0);
    __syncthreads();
    p1_scan_a(sc, H1, tid);
    Out o0;
    p1_out_read(o0, tid);
    const auto rs0 = __builtin_amdgcn_make_buffer_rsrc((void*)(rec + (size_t)c0 * (CHP2 * 12)), 0, (u32)CHP2 * 12u, 0x00020000);
    p1_out_store(o0, 0, 2, rs0, total0, tid);
    __builtin_amdgcn_sched_barrier(0);
    __syncthreads();
    const u32 total1 = p1_scan_b(sc, H1, tid);
    p1_out_store(o0, 2, 4, rs0, total0, tid);
    __builtin_amdgcn_sched_barrier(0);
    __syncthreads();
    p1_store_tab(tab, c1, tid);
#pragma unroll
    for (int u = 0; u < 5; ++u) {
        if (u < 4 || tid < QLAST) {
            u32 pos[4];
#pragma unroll
            for (int r = 0; r < 4; ++r) pos[r] = lds_add32_rtn(H1 + (hi[4 * u + r] >> 16), 1u);
#pragma unroll
            for (int r = 0; r < 4; ++r) {
                *L32(L1_LO + pos[r] * 4) = lo[4 * u + r];
                *L16(L1_C + pos[r] * 2) = (u16)hi[4 * u + r];
            }
        }
        p1_out_store(o0, 4 + (u * 7 + 2) / 5, 4 + ((u + 1) * 7 + 2) / 5, rs0, total0, tid);
        __builtin_amdgcn_sched_barrier(0);
    }
    {
        u32 mx = 0;
#pragma unroll
        for (int u = 0; u < 3; ++u)
#pragma unroll
            for (int k = 0; k < 4; ++k) mx = max(mx, max(mv[u][k] & 0x7fffu, (mv[u][k] >> 16) & 0x7fffu));
        mx = wave_scan_max(mx);
        if ((tid & 63) == 63) __hip_atomic_fetch_max(L32(L1_W + 64), mx, __ATOMIC_RELAXED, __HIP_MEMORY_SCOPE_WORKGROUP);
        if (nanacc != nanacc) *L32(L1_W + 68) = 1u;
    }
    __syncthreads();
    {
        Out o1;
        p1_out_read(o1, tid);
        const auto rs1 = __builtin_amdgcn_make_buffer_rsrc((void*)(rec + (size_t)c1 * (CHP2 * 12)), 0, (u32)CHP2 * 12u, 0x00020000);
        p1_out_store(o1, 0, NOUT, rs1, total1, tid);
    }
    if (tid == 0) meta[blockIdx.x] = *L32(L1_W + 64) | (*L32(L1_W + 68) << 16);
    if (blockIdx.x == 1 && tid < NNULL) {
        const u32 row = RB + ((tid >> 2) & 15);
        u32* p = (u32*)(rec + ((size_t)NULLP + tid) * 12);
        p[0] = row << 16; p[1] = 0u; p[2] = row;
    }
}


struct EpiW { s16x4 wd, wu, ws, wr; u32 cen; };

__device__ __forceinline__ u32 pack_bf(u32 a, u32 b) { return a | (b << 16); }

__device__ __forceinline__ void epi_weights(EpiW& w, const u16* __restrict__ w_rbf, const u16* __restrict__ w_sbf,
                                            const u16* __restrict__ w_down, const u16* __restrict__ w_up,
                                            const u16* __restrict__ centers, int lane) {
    const int fo = lane & 15, g = lane >> 4;
    w.wd = __builtin_bit_cast(s16x4, (u32x2){pack_bf(w_down[(4 * g + 0) * 16 + fo], w_down[(4 * g + 1) * 16 + fo]),
                                             pack_bf(w_down[(4 * g + 2) * 16 + fo], w_down[(4 * g + 3) * 16 + fo])});
    w.wu = __builtin_bit_cast(s16x4, (u32x2){pack_bf(w_up[(4 * g + 0) * 16 + fo], w_up[(4 * g + 1) * 16 + fo]),
                                             pack_bf(w_up[(4 * g + 2) * 16 + fo], w_up[(4 * g + 3) * 16 + fo])});
    const u32 s01 = pack_bf(w_sbf[(2 * g) * 16 + fo], w_sbf[(2 * g + 1) * 16 + fo]);
    const u32 r01 = pack_bf(w_rbf[(2 * g) * 16 + fo], w_rbf[(2 * g + 1) * 16 + fo]);
    w.ws = __builtin_bit_cast(s16x4, (u32x2){s01, s01});
    w.wr = __builtin_bit_cast(s16x4, (u32x2){r01, r01});
    w.cen = pack_bf(centers[2 * g], centers[2 * g + 1]);
}

__device__ __forceinline__ void epi_gates(const EpiW& w, u32 dbits, f32x4& sg, f32x4& gt) {
    const f32x4 z = {0.f, 0.f, 0.f, 0.f};
    const float KE = -14.426950408889634f;
    const float d = bf2f(dbits);
    const float x0 = d - __uint_as_float(w.cen << 16), x1 = d - __uint_as_float(w.cen & 0xffff0000u);
    const float r0 = __builtin_amdgcn_exp2f(x0 * KE * x0), r1 = __builtin_amdgcn_exp2f(x1 * KE * x1);
    const u32 rh = cvt_pk_bf16(r0, r1);
    const u32 rl = cvt_pk_bf16(r0 - __uint_as_float(rh << 16), r1 - __uint_as_float(rh & 0xffff0000u));
    const s16x4 rb = __builtin_bit_cast(s16x4, (u32x2){rh, rl});
    sg = __builtin_amdgcn_mfma_f32_16x16x16bf16_1k(w.ws, rb, z, 0, 0, 0);
    gt = __builtin_amdgcn_mfma_f32_16x16x16bf16_1k(w.wr, rb, z, 0, 0, 0);
}
__device__ __forceinline__ u32x2 epi_rows(const EpiW& w, float a0, float a1, float a2, float a3, float scale,
                                          const f32x4& sg, const f32x4& gt, u32x2 mv) {
    const f32x4 z = {0.f, 0.f, 0.f, 0.f};
    const s16x4 av = __builtin_bit_cast(s16x4, (u32x2){cvt_pk_bf16(a0, a1), cvt_pk_bf16(a2, a3)});
    const f32x4 md = __builtin_amdgcn_mfma_f32_16x16x16bf16_1k(w.wd, av, z, 0, 0, 0);
    const f32x4 ss = sg * scale;
    const s16x4 ag = __builtin_bit_cast(s16x4, (u32x2){cvt_pk_bf16(md[0] * ss[0], md[1] * ss[1]),
                                                       cvt_pk_bf16(md[2] * ss[2], md[3] * ss[3])});
    const f32x4 u = __builtin_amdgcn_mfma_f32_16x16x16bf16_1k(w.wu, ag, z, 0, 0, 0);
    const float o0 = __builtin_fmaf(u[0], gt[0], __uint_as_float(mv[0] << 16));
    const float o1 = __builtin_fmaf(u[1], gt[1], __uint_as_float(mv[0] & 0xffff0000u));
    const float o2 = __builtin_fmaf(u[2], gt[2], __uint_as_float(mv[1] << 16));
    const float o3 = __builtin_fmaf(u[3], gt[3], __uint_as_float(mv[1] & 0xffff0000u));
    return (u32x2){cvt_pk_bf16(o0, o1), cvt_pk_bf16(o2, o3)};
}

__device__ __forceinline__ void decode_pair(u32 alo, u32 ahi, u32 ckl, u32 ckh, float& f0, float& f1) {
    u32 borrow;
    const u32 slo = __builtin_subc(alo, ckl, 0u, &borrow);
    const u32 shi = ahi - ckh - borrow - ckl + (slo >> 31);
    const float fs = (float)(int)slo, fd = (float)(int)shi;
    f0 = fs + fd;
    f1 = fs - fd;
}


struct FE {
    u32 val, cumv;
    u32 n[S];
    u32 rounds[S];
    u32 s, r;
    u32 k00, cap;
    u32 carry, mark, tabl;
    u32 cbase;
};
struct RegSet { u32x3 d[NSB]; u32 nw, soff; bool live; };

__device__ __forceinline__ void fe_fetch(FE& f, RegSet& R, int lane, __amdgpu_buffer_rsrc_t rsr) {
    R.live = f.s < (u32)S;
    const u32 n0 = f.n[0];
    const u32 k00 = f.k00;
    R.nw = R.live ? min(n0 - min(n0, k00), f.cap) : 0u;
    R.soff = min(f.s, (u32)S - 1) * SBYTES;
    if (R.nw == 0) {
        R.d[0] = (u32x3){(u32)(RB + (lane >> 2)) << 16, 0u, (u32)(RB + (lane >> 2))};
        return;
    }
    if (f.r == 0) {
        const u32 t = *(volatile lds_u32*)(size_t)(f.tabl + f.s * 256);
        const u32 len = t >> 16, start = t & 0xffffu;
        const u32 cum = wave_scan_add(len) - len;
        f.val = (lane == 32 ? NULLP - n0 : f.cbase + start - cum) + (f.s << 24);
        f.cumv = lane == 32 ? n0 : (len ? cum : 0x7fffffffu);
    }
    const u32 slot = min(f.cumv - k00, (u32)CAPW + lane);
    *(volatile lds_u32*)(size_t)(f.mark + slot * 4) = f.val;
    u32 v[NSB];
#pragma unroll
    for (int j = 0; j < NSB; ++j) v[j] = *(volatile lds_u32*)(size_t)(f.mark + (j * 64 + lane) * 4);
#pragma unroll
    for (int j = 0; j < NSB; ++j) v[j] = wave_scan_max(v[j]);
    u32 run = f.carry;
#pragma unroll
    for (int j = 0; j < NSB; ++j) {
        v[j] = max(v[j], run);
        run = __builtin_amdgcn_readlane(v[j], 63);
        if ((u32)(j * 64 + 64) <= f.cap) f.carry = run;
    }
#pragma unroll
    for (int j = 0; j < NSB; ++j) {
        if (j == 0 || (u32)(j * 64) < R.nw) {
            const u32 idx = (v[j] & 0xffffffu) + k00 + j * 64 + lane;
            R.d[j] = __builtin_amdgcn_raw_buffer_load_b96(rsr, __umul24(idx, 12u), 0, 0);
        }
    }
}
__device__ __forceinline__ void fe_advance(FE& f) {
    if (f.s >= (u32)S) return;
    f.r += 1;
    f.k00 += f.cap;
    f.cap = CAPW;
    if (f.r >= f.rounds[0]) {
#pragma unroll
        for (int s = 0; s + 1 < S; ++s) { f.n[s] = f.n[s + 1]; f.rounds[s] = f.rounds[s + 1]; }
        f.r = 0;
        f.k00 = 0;
        f.s += 1;
    }
}

struct Gat { u32x2 x0, x1, x2, x3; u32 la, b2; };
struct P2C { u32 q8, q16, psc2; __amdgpu_buffer_rsrc_t rsm; };

__device__ __forceinline__ Gat gat_issue(u32 lo, u32 b2, u32 soff, const P2C& k) {
    Gat g;
    const u32 ga = (lo & 0xffffu) << 5, row = lo >> 16;
    g.x0 = __builtin_amdgcn_raw_buffer_load_b64(k.rsm, quad_bcast<0>(ga) + k.q8, soff, 0);
    g.x1 = __builtin_amdgcn_raw_buffer_load_b64(k.rsm, quad_bcast<1>(ga) + k.q8, soff, 0);
    g.x2 = __builtin_amdgcn_raw_buffer_load_b64(k.rsm, quad_bcast<2>(ga) + k.q8, soff, 0);
    g.x3 = __builtin_amdgcn_raw_buffer_load_b64(k.rsm, quad_bcast<3>(ga) + k.q8, soff, 0);
    g.la = (row << 6) | ((row << 2) & 8u);
    g.b2 = b2;
    lds_add32(L2_CNT + row * 4, 1u);
    asm volatile("" : "+v"(g.la), "+v"(g.b2));
    __builtin_amdgcn_sched_barrier(0);
    return g;
}

__device__ __forceinline__ void gat_consume(const Gat& g, u32 q16) {
    auto step = [&](u32x2 x, u32 laj, u32 bj) {
        float s0, d0, s1, d1;
        asm("v_dot2_f32_bf16 %0, %1, %2, %3" : "=v"(s0) : "v"(x[0]), "v"(bj), "s"(KMAGIC));
        asm("v_dot2_f32_bf16 %0, %1, %2, %3 neg_hi:[0,1,0]" : "=v"(d0) : "v"(x[0]), "v"(bj), "s"(KMAGIC));
        asm("v_dot2_f32_bf16 %0, %1, %2, %3" : "=v"(s1) : "v"(x[1]), "v"(bj), "s"(KMAGIC));
        asm("v_dot2_f32_bf16 %0, %1, %2, %3 neg_hi:[0,1,0]" : "=v"(d1) : "v"(x[1]), "v"(bj), "s"(KMAGIC));
        const u32 a0 = laj + q16;
        lds_add64(a0, ((u64)__float_as_uint(d0) << 32) | __float_as_uint(s0));
        lds_add64(a0 ^ 8u, ((u64)__float_as_uint(d1) << 32) | __float_as_uint(s1));
    };
    step(g.x0, quad_bcast<0>(g.la), quad_bcast<0>(g.b2));
    step(g.x1, quad_bcast<1>(g.la), quad_bcast<1>(g.b2));
    step(g.x2, quad_bcast<2>(g.la), quad_bcast<2>(g.b2));
    step(g.x3, quad_bcast<3>(g.la), quad_bcast<3>(g.b2));
    __builtin_amdgcn_sched_barrier(0);
}

struct Half { u32 loa, lob, b2a, b2b; };
template <int J>
__device__ __forceinline__ Half sb_decode(const RegSet& R, const P2C& k) {
    u32x3 d = R.d[J];
    asm volatile("" : "+v"(d));
    const u32 cp = (d[1] & 0xffffu) | (d[2] & 0xffff0000u);
    const u32 cs = __builtin_bit_cast(u32, __builtin_bit_cast(u16x2, cp) + __builtin_bit_cast(u16x2, k.psc2));
    Half h;
    h.loa = d[0];
    h.lob = __builtin_amdgcn_alignbit(d[2], d[1], 16);
    h.b2a = __builtin_amdgcn_perm(cs, cs, 0x01000100u);
    h.b2b = __builtin_amdgcn_perm(cs, cs, 0x03020302u);
    return h;
}

template <int J>
__device__ __forceinline__ void sb_step(const RegSet& R, u32 nsb, const Half& h, const Gat& g0, const P2C& k) {
    const Gat g1 = gat_issue(h.lob, h.b2b, R.soff, k);
    gat_consume(g0, k.q16);
    if constexpr (J + 1 < NSB) {
        if (nsb > (u32)(J + 1)) {
            const Half hn = sb_decode<J + 1>(R, k);
            if (R.nw <= (u32)((J + 1) * 64 + 32)) {
                const auto ml = __builtin_amdgcn_permlane32_swap(hn.loa, hn.lob, false, false);
                const auto mb = __builtin_amdgcn_permlane32_swap(hn.b2a, hn.b2b, false, false);
                const Gat gn = gat_issue(ml[0], mb[0], R.soff, k);
                gat_consume(g1, k.q16);
                gat_consume(gn, k.q16);
                return;
            }
            const Gat gn = gat_issue(hn.loa, hn.b2a, R.soff, k);
            gat_consume(g1, k.q16);
            sb_step<J + 1>(R, nsb, hn, gn, k);
            return;
        }
    }
    gat_consume(g1, k.q16);
}

__global__ __launch_bounds__(1024) void p2_accumulate(const char* __restrict__ rec, const u32* __restrict__ tab,
                                                      const u32* __restrict__ meta, const u16* __restrict__ m,
                                                      const u16* __restrict__ dist, const u16* __restrict__ centers,
                                                      const u16* __restrict__ w_rbf, const u16* __restrict__ w_sbf,
                                                      const u16* __restrict__ w_down, const u16* __restrict__ w_up,
                                                      u16* __restrict__ out) {
    const int tid = threadIdx.x, lane = tid & 63;
    const int wave = __builtin_amdgcn_readfirstlane(tid >> 6);
    const int b = (blockIdx.x & 7) * 32 + (blockIdx.x >> 3);
    const int row0 = b * RB;
    const auto rsr = __builtin_amdgcn_make_buffer_rsrc((void*)rec, 0, (u32)(WS_TAB), 0x00020000);
    const auto rsm = __builtin_amdgcn_make_buffer_rsrc((void*)m, 0, (u32)E_ * 32u, 0x00020000);
    const auto rst = __builtin_amdgcn_make_buffer_rsrc((void*)tab, 0, (u32)NC * NB * S * 4u, 0x00020000);
    const auto rsq = __builtin_amdgcn_make_buffer_rsrc((void*)meta, 0, 1024u, 0x00020000);
    const int c = (lane & 31) * 16 + wave;
    const u32 toff = lane < 32 ? (u32)(c * NB + b) * (S * 4) : 0x80000000u;
    u32 t[S];
    {
        const u32x4 t0 = __builtin_amdgcn_raw_buffer_load_b128(rst, toff, 0, 0);
        t[0] = t0[0]; t[1] = t0[1]; t[2] = t0[2]; t[3] = t0[3];
        t[4] = __builtin_amdgcn_raw_buffer_load_b32(rst, toff + 16, 0, 0);
    }
    const u32x4 mt4 = __builtin_amdgcn_raw_buffer_load_b128(rsq, lane * 16, 0, 0);
    __builtin_amdgcn_sched_barrier(0);
    const u32 mark = L2_MARK + wave * ((CAPW + 64) * 4);
    *(lds_u32x4*)(size_t)(mark + lane * 16) = (u32x4){0u, 0u, 0u, 0u};
    if (lane < 32) *(lds_u32x4*)(size_t)(mark + 1024 + lane * 16) = (u32x4){0u, 0u, 0u, 0u};
    static_assert((CAPW + 64) * 4 == 1536, "marker area");
    for (u32 a = tid * 16; a < L2_MARK; a += 1024 * 16) *(lds_u32x4*)(size_t)a = (u32x4){0u, 0u, 0u, 0u};
    if (tid == 0) *L32(L2_FLAG) = 0u;
    const u32 mtm = max(max(mt4[0] & 0xffffu, mt4[1] & 0xffffu), max(mt4[2] & 0xffffu, mt4[3] & 0xffffu));
    const u32 mmax = __builtin_amdgcn_readlane(wave_scan_max(mtm), 63);
    const bool anynan = __builtin_amdgcn_ballot_w64(((mt4[0] | mt4[1] | mt4[2] | mt4[3]) & 0x10000u) != 0) != 0;
    const u32 eb = mmax >> 7;
    const bool fast = !anynan && (mmax == 0 || (eb >= 64 && eb <= 147));
    const u32 ebc = mmax == 0 ? 127u : min(max(eb, 64u), 147u);
    const u32 pexp = 147u - ebc;
    const float inv2 = __uint_as_float((126u - pexp) << 23);
    const int e = lane & 15, g = lane >> 4;
    EpiW w;
    bool slow = !fast;
    if (fast) {
        FE f;
        f.tabl = L2_TABW + wave * (S * 256) + lane * 4;
        f.cbase = (u32)c * CHP2;
#pragma unroll
        for (int s = 0; s < S; ++s) {
            f.n[s] = __builtin_amdgcn_readlane(wave_scan_add(t[s] >> 16), 63);
            *L32(f.tabl + s * 256) = t[s];
        }
        f.s = 0; f.r = 0; f.carry = 0; f.mark = mark;
        f.k00 = 0; f.cap = CAP0;
        P2C k;
        k.q8 = (lane & 3) * 8; k.q16 = (lane & 3) * 16 + L2_ACC;
        k.psc2 = (pexp << 7) * 0x10001u; k.rsm = rsm;
        RegSet cur, nxt;
        fe_fetch(f, nxt, lane, rsr);
        if (lane < S) {
            u32 v = f.n[0];
#pragma unroll
            for (int s = 1; s < S; ++s) v = lane == s ? f.n[s] : v;
            *L32(L2_NW + lane * 64 + wave * 4) = v;
        }
        cur = nxt;
#pragma unroll
        for (int j = 0; j < NSB; ++j) asm volatile("" : "+v"(cur.d[j]));
        __syncthreads();
#pragma unroll
        for (int s = 0; s < S; ++s) {
            const u32 v = *L32(L2_NW + s * 64 + (lane & 15) * 4);
            const u32 mx = __builtin_amdgcn_readlane(wave_scan_max(v), 63);
            f.rounds[s] = s == 0 ? 1u + (mx - min(mx, (u32)CAP0) + CAPW - 1) / CAPW : max((mx + CAPW - 1) / CAPW, 1u);
        }
        fe_advance(f);
        Half h = sb_decode<0>(cur, k);
        Gat g0 = gat_issue(h.loa, h.b2a, cur.soff, k);
        fe_fetch(f, nxt, lane, rsr);
        fe_advance(f);
        if (wave == 0) {
            epi_weights(w, w_rbf, w_sbf, w_down, w_up, centers, lane);
            *(lds_u32x2*)(size_t)(L2_WD + lane * 8) = __builtin_bit_cast(u32x2, w.wd);
            *(lds_u32x2*)(size_t)(L2_WU + lane * 8) = __builtin_bit_cast(u32x2, w.wu);
            *(lds_u32x2*)(size_t)(L2_WS + lane * 8) = __builtin_bit_cast(u32x2, w.ws);
            *(lds_u32x2*)(size_t)(L2_WR + lane * 8) = __builtin_bit_cast(u32x2, w.wr);
            *L32(L2_CEN + lane * 4) = w.cen;
        } else if (wave < 4) {
#pragma unroll
            for (int u = 0; u < 4; ++u) {
                const int r = ((wave - 1) * 4 + u) * 128 + lane * 2;
                if (r < RB) *L32(L2_DIST + r * 2) = *(const u32*)(dist + row0 + r);
            }
        }
#pragma clang loop unroll(disable)
        while (cur.live) {
            sb_step<0>(cur, (cur.nw + 63) >> 6, h, g0, k);
            cur = nxt;
#pragma unroll
            for (int j = 0; j < NSB; ++j) asm volatile("" : "+v"(cur.d[j]));
            h = sb_decode<0>(cur, k);
            g0 = gat_issue(h.loa, h.b2a, cur.soff, k);
            fe_fetch(f, nxt, lane, rsr);
            fe_advance(f);
            __syncthreads();
        }
        asm volatile("" :: "v"(g0.x0), "v"(g0.x1), "v"(g0.x2), "v"(g0.x3));
        const auto rso = __builtin_amdgcn_make_buffer_rsrc((void*)(out + (size_t)row0 * 16), 0, (u32)RB * 32u, 0x00020000);
        const auto rsi = __builtin_amdgcn_make_buffer_rsrc((void*)(m + (size_t)row0 * 16), 0, (u32)RB * 32u, 0x00020000);
        u32x2 mv[5];
        f32x4 sg[5], gt[5];
#pragma unroll
        for (int u = 0; u < 5; ++u) mv[u] = __builtin_amdgcn_raw_buffer_load_b64(rsi, ((wave + 16 * u) * 16 + e) * 32 + g * 8, 0, 0);
        w.wd = __builtin_bit_cast(s16x4, *(lds_u32x2*)(size_t)(L2_WD + lane * 8));
        w.wu = __builtin_bit_cast(s16x4, *(lds_u32x2*)(size_t)(L2_WU + lane * 8));
        w.ws = __builtin_bit_cast(s16x4, *(lds_u32x2*)(size_t)(L2_WS + lane * 8));
        w.wr = __builtin_bit_cast(s16x4, *(lds_u32x2*)(size_t)(L2_WR + lane * 8));
        w.cen = *L32(L2_CEN + lane * 4);
#pragma unroll
        for (int u = 0; u < 5; ++u) {
            const int rc = min((wave + 16 * u) * 16 + e, RB - 1);
            epi_gates(w, *L16(L2_DIST + rc * 2), sg[u], gt[u]);
        }
        u32 over = 0;
#pragma unroll
        for (int u = 0; u < 5; ++u) {
            const int r = (wave + 16 * u) * 16 + e;
            const int rc = min(r, RB - 1);
            const u32 aa = L2_ACC + rc * 64 + g * 16 + ((rc << 2) & 8);
            const u32x2 p0 = *(lds_u32x2*)(size_t)aa, p1 = *(lds_u32x2*)(size_t)(aa ^ 8);
            const u32 cn = *L32(L2_CNT + rc * 4);
            over = max(over, cn);
            const u32 q = __umul24(cn, KBITS >> 22);
            const u32 ckl = q << 22, ckh = q >> 10;
            float a0, a1, a2, a3;
            decode_pair(p0[0], p0[1], ckl, ckh, a0, a1);
            decode_pair(p1[0], p1[1], ckl, ckh, a2, a3);
            const u32x2 o = epi_rows(w, a0, a1, a2, a3, inv2, sg[u], gt[u], mv[u]);
            __builtin_amdgcn_raw_buffer_store_b64(o, rso, r * 32 + g * 8, 0, AUX_WT);
        }
        if (over > 511u) *L32(L2_FLAG) = 1u;
        asm volatile("s_waitcnt vmcnt(0)" ::: "memory");
        __syncthreads();
        slow = *L32(L2_FLAG) != 0;
    }
    if (!slow) return;
    const u32x2* m2 = (const u32x2*)m;
    u32x2* out2 = (u32x2*)out;
    epi_weights(w, w_rbf, w_sbf, w_down, w_up, centers, lane);
    for (int half = 0; half < 2; ++half) {
        __syncthreads();
        for (u32 a = tid * 16; a < 625 * 128; a += 1024 * 16) *(lds_u32x4*)(size_t)a = (u32x4){0u, 0u, 0u, 0u};
        __syncthreads();
        for (int s = 0; s < S; ++s) {
            for (int i = 0; i < 32; ++i) {
                const int cc = i * 16 + wave;
                const u32 tt = tab[((size_t)cc * NB + b) * S + s];
                const u32 len = (tt >> 16) * 2, start = (tt & 0xffffu) * 2;
                for (u32 j0 = 0; j0 < len; j0 += 4) {
                    const u32 j = j0 + (lane >> 4);
                    if (j < len) {
                        const u16* rp = (const u16*)(rec + ((size_t)cc * CHP2 * 2 + start + j) * 6);
                        const u32 il = rp[0], ol = rp[1], cb = rp[2];
                        const int r = (int)ol - half * 625;
                        if (r >= 0 && r < 625) {
                            const float x = bf2f(m[((size_t)s * SR + il) * 16 + (lane & 15)]);
                            __hip_atomic_fetch_add((lds_f64*)(size_t)(r * 128 + (lane & 15) * 8), (double)(bf2f(cb) * x),
                                                   __ATOMIC_RELAXED, __HIP_MEMORY_SCOPE_WORKGROUP);
                        }
                    }
                }
            }
        }
        __syncthreads();
        for (int gi = wave; gi < 40; gi += 16) {
            const int r = gi * 16 + e;
            const bool valid = r < 625;
            const int rc = valid ? r : 624;
            const int grow = row0 + half * 625 + rc;
            const lds_f64* p = (const lds_f64*)(size_t)(rc * 128 + g * 32);
            f32x4 sg, gt;
            epi_gates(w, dist[grow], sg, gt);
            const u32x2 o = epi_rows(w, (float)p[0], (float)p[1], (float)p[2], (float)p[3], 1.0f, sg, gt, m2[(size_t)grow * 4 + g]);
            if (valid) out2[(size_t)grow * 4 + g] = o;
        }
    }
}

}

extern "C" void kernel_launch(void* const* d_in, const int* in_sizes, int n_in, void* d_out, int out_size, void* d_ws,
                              size_t ws_size, hipStream_t stream) {
    const u16* m = (const u16*)d_in[0];
    const int* idx_in = (const int*)d_in[1];
    const int* idx_out = (const int*)d_in[2];
    const u16* dist = (const u16*)d_in[3];
    const u16* angle = (const u16*)d_in[4];
    const u16* centers = (const u16*)d_in[5];
    const u16* ang_freq = (const u16*)d_in[6];
    const u16* w_rbf = (const u16*)d_in[7];
    const u16* w_sbf = (const u16*)d_in[8];
    const u16* w_down = (const u16*)d_in[9];
    const u16* w_up = (const u16*)d_in[10];
    char* ws = (char*)d_ws;
    (void)hipFuncSetAttribute((const void*)p1_partition, hipFuncAttributeMaxDynamicSharedMemorySize, L1_SIZE);
    (void)hipFuncSetAttribute((const void*)p2_accumulate, hipFuncAttributeMaxDynamicSharedMemorySize, L2_SIZE);
    p1_partition<<<NWG1, 1024, L1_SIZE, stream>>>((const u32x4*)idx_in, (const u32x4*)idx_out, (const u32x2*)angle, ang_freq,
                                                  (const u32x4*)m, ws + WS_REC, (u32*)(ws + WS_TAB), (u32*)(ws + WS_META));
    p2_accumulate<<<NB, 1024, L2_SIZE, stream>>>(ws + WS_REC, (const u32*)(ws + WS_TAB), (const u32*)(ws + WS_META), m, dist,
                                                 centers, w_rbf, w_sbf, w_down, w_up, (u16*)d_out);
}
```

```cpp
#include <hip/hip_runtime.h>
#include <hip/hip_bf16.h>
#include <stdint.h>

typedef unsigned int u32;
typedef unsigned long long u64;
typedef unsigned short u16;
using u16x2 = __attribute__((ext_vector_type(2))) unsigned short;
using u32x2 = __attribute__((ext_vector_type(2))) unsigned;
using u32x3 = __attribute__((ext_vector_type(3))) unsigned;
using u32x4 = __attribute__((ext_vector_type(4))) unsigned;
using f32x2 = __attribute__((ext_vector_type(2))) float;
using f32x4 = __attribute__((ext_vector_type(4))) float;
using bf16x2 = __attribute__((ext_vector_type(2))) __bf16;
using s16x4 = __attribute__((ext_vector_type(4))) short;
typedef __attribute__((address_space(3))) u64 lds_u64;
typedef __attribute__((address_space(3))) u32 lds_u32;
typedef __attribute__((address_space(3))) u16 lds_u16;
typedef __attribute__((address_space(3))) double lds_f64;
typedef __attribute__((address_space(3))) u32x2 lds_u32x2;
typedef __attribute__((address_space(3))) u32x4 lds_u32x4;

namespace {

constexpr int E_ = 320000;
constexpr int T_ = 10240000;
constexpr int NB = 256;
constexpr int RB = 1250;
constexpr int S = 5;
constexpr int SR = 65536;
constexpr u32 SBYTES = (u32)SR * 32u;
constexpr int CH = 20000;
constexpr int QLAST = CH / 4 - 4096;
constexpr int CHP2 = (CH + NB * S) / 2;
constexpr int NC = 512;
constexpr int NWG1 = 256;
constexpr u32 MAGIC_RB = 3435974u;
static_assert(NC * CH == T_ && NC == 2 * NWG1 && NC == 512 && NWG1 == 256 && NB * RB == E_ && S * SR >= E_, "shapes");

constexpr u32 NULLP = (u32)NC * CHP2;
constexpr int NNULL = 512;
constexpr size_t WS_REC = 0;
constexpr size_t WS_TAB = ((size_t)NULLP + NNULL) * 12 + 2048;
constexpr size_t WS_META = WS_TAB + (size_t)NC * NB * S * 4;
static_assert(WS_TAB % 128 == 0 && WS_META % 128 == 0, "alignment");

constexpr u32 L1_LO = 0;
constexpr u32 L1_C = CHP2 * 8;
constexpr u32 L1_H = CHP2 * 12;
constexpr u32 L1_TAB = L1_H + 16384;
constexpr u32 L1_W = L1_TAB + NB * S * 4;
constexpr u32 L1_SIZE = L1_W + 128;

constexpr int NSB = 5;
constexpr int CAPW = NSB * 64;
constexpr int CAP0 = 128;
constexpr u32 L2_CNT = 0;
constexpr u32 L2_ACC = 5120;
constexpr u32 L2_MARK = L2_ACC + (RB + 16) * 64;
constexpr u32 L2_FLAG = L2_MARK + 16 * (CAPW + 64) * 4;
constexpr u32 L2_NW = L2_FLAG + 64;
constexpr u32 L2_TABW = L2_NW + 8 * 64;
constexpr u32 L2_WD = L2_TABW + 16 * S * 256;
constexpr u32 L2_WU = L2_WD + 512;
constexpr u32 L2_WS = L2_WU + 512;
constexpr u32 L2_WR = L2_WS + 512;
constexpr u32 L2_CEN = L2_WR + 512;
constexpr u32 L2_DIST = L2_CEN + 256;
constexpr u32 L2_SIZE = L2_DIST + 2560;
static_assert(NNULL >= CAPW + 64 && NULLP + NNULL < (1u << 24), "null records");
static_assert(L2_SIZE <= 163840 && L1_SIZE <= 163840 && L2_SIZE % 16 == 0, "LDS");

constexpr int AUX_NT = 2;
constexpr int AUX_WT = 16;

constexpr float KMAGIC = 12582912.0f;
constexpr u32 KBITS = 0x4B400000u;

__device__ __forceinline__ lds_u32* L32(u32 a) { return (lds_u32*)(size_t)a; }
__device__ __forceinline__ lds_u64* L64(u32 a) { return (lds_u64*)(size_t)a; }
__device__ __forceinline__ lds_u16* L16(u32 a) { return (lds_u16*)(size_t)a; }
__device__ __forceinline__ void lds_add32(u32 a, u32 v) { __hip_atomic_fetch_add(L32(a), v, __ATOMIC_RELAXED, __HIP_MEMORY_SCOPE_WORKGROUP); }
__device__ __forceinline__ u32 lds_add32_rtn(u32 a, u32 v) { return __hip_atomic_fetch_add(L32(a), v, __ATOMIC_RELAXED, __HIP_MEMORY_SCOPE_WORKGROUP); }
__device__ __forceinline__ void lds_add64(u32 a, u64 v) { __hip_atomic_fetch_add(L64(a), v, __ATOMIC_RELAXED, __HIP_MEMORY_SCOPE_WORKGROUP); }
__device__ __forceinline__ float bf2f(u32 v) { return __uint_as_float(v << 16); }

template <int CTRL, int RM = 0xf, bool BC = true>
__device__ __forceinline__ u32 dpp0(u32 v) { return (u32)__builtin_amdgcn_update_dpp(0, (int)v, CTRL, RM, 0xf, BC); }
template <int J>
__device__ __forceinline__ u32 quad_bcast(u32 v) { return (u32)__builtin_amdgcn_mov_dpp((int)v, J * 0x55, 0xf, 0xf, true); }

__device__ __forceinline__ u32 wave_scan_add(u32 v) {
    v += dpp0<0x111>(v); v += dpp0<0x112>(v); v += dpp0<0x114>(v); v += dpp0<0x118>(v);
    v += dpp0<0x142, 0xa, false>(v); v += dpp0<0x143, 0xc, false>(v);
    return v;
}
__device__ __forceinline__ u32 wave_scan_max(u32 v) {
    v = max(v, dpp0<0x111>(v)); v = max(v, dpp0<0x112>(v)); v = max(v, dpp0<0x114>(v)); v = max(v, dpp0<0x118>(v));
    v = max(v, dpp0<0x142, 0xa, false>(v)); v = max(v, dpp0<0x143, 0xc, false>(v));
    return v;
}
__device__ __forceinline__ u32 cvt_pk_bf16(float lo, float hi) {
    return __builtin_bit_cast(u32, __builtin_convertvector((f32x2){lo, hi}, bf16x2));
}

struct Freq { float hi, lo; };
__device__ __forceinline__ Freq make_freq(float f) {
    const float C_HI = 0.15915493667125702f;
    const float C_LO = 6.4206382985757765e-09f;
    Freq r;
    r.hi = f * C_HI;
    r.lo = __builtin_fmaf(f, C_LO, __builtin_fmaf(f, C_HI, -r.hi));
    return r;
}
__device__ __forceinline__ float cos_rev(float a, const Freq& f) {
    const float n = __builtin_rintf(a * f.hi);
    float r = __builtin_fmaf(a, f.hi, -n);
    r = __builtin_fmaf(a, f.lo, r);
    return __builtin_amdgcn_cosf(r);
}


struct Raw { u32x4 o[5]; u32x4 i[5]; u32x2 a[5]; };
struct P1In { __amdgpu_buffer_rsrc_t io, ii, an; };

__device__ __forceinline__ void p1_load_group(Raw& r, int u, const P1In& in, int chunk, int tid) {
    u32 q = chunk * (CH / 4) + u * 1024 + tid;
    if (u == 4 && tid >= QLAST) q = 0x0ffffff0u;
    r.o[u] = __builtin_amdgcn_raw_buffer_load_b128(in.io, q * 16, 0, AUX_NT);
    r.i[u] = __builtin_amdgcn_raw_buffer_load_b128(in.ii, q * 16, 0, AUX_NT);
    r.a[u] = __builtin_amdgcn_raw_buffer_load_b64(in.an, q * 8, 0, AUX_NT);
    __builtin_amdgcn_sched_barrier(0);
}

__device__ __forceinline__ void p1_convert(const Raw& raw, int u, u32 H, const Freq& freq, float& nanacc, u32* lo, u32* hi) {
#pragma unroll
    for (int j = 0; j < 4; ++j) {
        const u32 o = raw.o[u][j], i = raw.i[u][j];
        const u32 aw = raw.a[u][j >> 1];
        const float a = (j & 1) ? __uint_as_float(aw & 0xffff0000u) : __uint_as_float(aw << 16);
        const float c = cos_rev(a, freq);
        nanacc = __builtin_fmaf(c, 0.0f, nanacc);
        const u32 b = (u32)(((u64)(o & 0xffffffu) * MAGIC_RB) >> 32);
        u32 ol;
        asm("v_mad_i32_i24 %0, %1, %2, %3" : "=v"(ol) : "v"(b), "s"(-RB), "v"(o));
        lo[j] = __builtin_amdgcn_perm(ol, i, 0x05040100u);
        hi[j] = ((((i >> 8) & 0xff00u) | b) << 18) | cvt_pk_bf16(c, 0.0f);
        lds_add32(H + (hi[j] >> 16), 1u);
    }
#pragma unroll
    for (int j = 0; j < 4; ++j) asm volatile("" : "+v"(hi[j]), "+v"(lo[j]));
    __builtin_amdgcn_sched_barrier(0);
}

struct Scan { u32x2 cnt; u32 p0, p1, sum, incl; };
__device__ __forceinline__ void p1_scan_a(Scan& sc, u32 H, int tid) {
    sc.cnt = *(lds_u32x2*)(size_t)(H + tid * 8);
    sc.p0 = (sc.cnt[0] + 1) & ~1u; sc.p1 = (sc.cnt[1] + 1) & ~1u;
    sc.sum = sc.p0 + sc.p1;
    sc.incl = wave_scan_add(sc.sum);
    if ((tid & 63) == 63) *L32(L1_W + (tid >> 6) * 4) = sc.incl;
}
__device__ __forceinline__ u32 p1_scan_b(const Scan& sc, u32 H, int tid) {
    const int lane = tid & 63;
    const u32 wraw = *L32(L1_W + (lane & 15) * 4);
    const u32 wsum = wave_scan_add(lane < 16 ? wraw : 0u);
    const u32 total = __builtin_amdgcn_readlane(wsum, 15);
    const u32 woff = __builtin_amdgcn_readlane(wsum - wraw, __builtin_amdgcn_readfirstlane(tid >> 6));
    const u32 st0 = woff + sc.incl - sc.sum, st1 = st0 + sc.p0;
    *(lds_u32x2*)(size_t)(H + tid * 8) = (u32x2){st0, st1};
    if (tid < NB * S / 2) {
        const int s = (2 * tid) >> 8, b = (2 * tid) & 255;
        *L32(L1_TAB + (b * S + s) * 4) = (st0 >> 1) | (sc.p0 << 15);
        *L32(L1_TAB + ((b + 1) * S + s) * 4) = (st1 >> 1) | (sc.p1 << 15);
        if (sc.cnt[0] & 1) { *L32(L1_LO + (st0 + sc.cnt[0]) * 4) = (u32)RB << 16; *L16(L1_C + (st0 + sc.cnt[0]) * 2) = 0; }
        if (sc.cnt[1] & 1) { *L32(L1_LO + (st1 + sc.cnt[1]) * 4) = (u32)RB << 16; *L16(L1_C + (st1 + sc.cnt[1]) * 2) = 0; }
    }
    return total;
}
__device__ __forceinline__ void p1_store_tab(u32* __restrict__ tab, int chunk, int tid) {
    if (tid < NB * S / 4) {
        const auto rtb = __builtin_amdgcn_make_buffer_rsrc((void*)(tab + (size_t)chunk * NB * S), 0, (u32)NB * S * 4u, 0x00020000);
        __builtin_amdgcn_raw_buffer_store_b128(*(lds_u32x4*)(size_t)(L1_TAB + tid * 16), rtb, tid * 16, 0, AUX_WT);
    }
}
constexpr int NOUT = (CHP2 + 1023) / 1024;
struct Out { u32x3 d[NOUT]; };
__device__ __forceinline__ void p1_out_read(Out& o, int tid) {
#pragma unroll
    for (int u = 0; u < NOUT; ++u) {
        const int j = u * 1024 + tid;
        const u32x2 l = *(lds_u32x2*)(size_t)(L1_LO + j * 8);
        const u32 cc = *L32(L1_C + j * 4);
        o.d[u] = (u32x3){l[0], __builtin_amdgcn_perm(l[1], cc, 0x05040100u), __builtin_amdgcn_perm(cc, l[1], 0x07060302u)};
    }
}
__device__ __forceinline__ void p1_out_store(const Out& o, int u0, int u1, __amdgpu_buffer_rsrc_t rso, u32 total, int tid) {
#pragma unroll
    for (int u = u0; u < u1; ++u) {
        if (u < NOUT) {
            const u32 j = u * 1024 + tid;
            const u32 off = (u < CH / 2048 || j < (total >> 1)) ? j * 12 : 0x80000000u;
            __builtin_amdgcn_raw_buffer_store_b96(o.d[u], rso, off, 0, AUX_WT);
        }
    }
}

__global__ __launch_bounds__(1024) void p1_partition(const u32x4* __restrict__ ii, const u32x4* __restrict__ io,
                                                     const u32x2* __restrict__ an, const u16* __restrict__ ang_freq,
                                                     const u32x4* __restrict__ m4, char* __restrict__ rec,
                                                     u32* __restrict__ tab, u32* __restrict__ meta) {
    constexpr u32 H0 = L1_H, H1 = L1_H + 8192;
    const int tid = threadIdx.x;
    const int c0 = blockIdx.x, c1 = blockIdx.x + NWG1;
    const u32 fbits = ang_freq[0];
    __builtin_amdgcn_sched_barrier(0);
    P1In in;
    in.io = __builtin_amdgcn_make_buffer_rsrc((void*)io, 0, (u32)T_ * 4u, 0x00020000);
    in.ii = __builtin_amdgcn_make_buffer_rsrc((void*)ii, 0, (u32)T_ * 4u, 0x00020000);
    in.an = __builtin_amdgcn_make_buffer_rsrc((void*)an, 0, (u32)T_ * 2u, 0x00020000);
    Raw raw;
#pragma unroll
    for (int u = 0; u < 5; ++u) p1_load_group(raw, u, in, c0, tid);
    *(lds_u32x4*)(size_t)(L1_H + tid * 16) = (u32x4){0u, 0u, 0u, 0u};
    if (tid < 2) *L32(L1_W + 64 + tid * 4) = 0u;
    const Freq freq = make_freq(bf2f(fbits));
    __syncthreads();
    float nanacc = 0.0f;
    u32 lo[20], hi[20];
#pragma unroll
    for (int u = 0; u < 5; ++u) {
        if (u < 4 || tid < QLAST) p1_convert(raw, u, H0, freq, nanacc, lo + 4 * u, hi + 4 * u);
        p1_load_group(raw, u, in, c1, tid);
    }
    __syncthreads();
    Scan sc;
    p1_scan_a(sc, H0, tid);
    __syncthreads();
    const u32 total0 = p1_scan_b(sc, H0, tid);
    __syncthreads();
    p1_store_tab(tab, c0, tid);
#pragma unroll
    for (int u = 0; u < 5; ++u) {
        if (u < 4 || tid < QLAST) {
            u32 pos[4];
#pragma unroll
            for (int r = 0; r < 4; ++r) pos[r] = lds_add32_rtn(H0 + (hi[4 * u + r] >> 16), 1u);
            u32 lo1[4], hi1[4];
            p1_convert(raw, u, H1, freq, nanacc, lo1, hi1);
#pragma unroll
            for (int r = 0; r < 4; ++r) {
                *L32(L1_LO + pos[r] * 4) = lo[4 * u + r];
                *L16(L1_C + pos[r] * 2) = (u16)hi[4 * u + r];
                lo[4 * u + r] = lo1[r];
                hi[4 * u + r] = hi1[r];
            }
        }
        __builtin_amdgcn_sched_barrier(0);
    }
    constexpr int PER = E_ * 2 / NWG1;
    u32x4 mv[3];
    mv[0] = m4[blockIdx.x * PER + tid];
    mv[1] = m4[blockIdx.x * PER + 1024 + tid];
    mv[2] = m4[blockIdx.x * PER + min(2048 + tid, PER - 1)];
    __builtin_amdgcn_sched_barrier(0);
    __syncthreads();
    p1_scan_a(sc, H1, tid);
    Out o0;
    p1_out_read(o0, tid);
    const auto rs0 = __builtin_amdgcn_make_buffer_rsrc((void*)(rec + (size_t)c0 * (CHP2 * 12)), 0, (u32)CHP2 * 12u, 0x00020000);
    p1_out_store(o0, 0, 2, rs0, total0, tid);
    __builtin_amdgcn_sched_barrier(0);
    __syncthreads();
    const u32 total1 = p1_scan_b(sc, H1, tid);
    p1_out_store(o0, 2, 4, rs0, total0, tid);
    __builtin_amdgcn_sched_barrier(0);
    __syncthreads();
    p1_store_tab(tab, c1, tid);
#pragma unroll
    for (int u = 0; u < 5; ++u) {
        if (u < 4 || tid < QLAST) {
            u32 pos[4];
#pragma unroll
            for (int r = 0; r < 4; ++r) pos[r] = lds_add32_rtn(H1 + (hi[4 * u + r] >> 16), 1u);
#pragma unroll
            for (int r = 0; r < 4; ++r) {
                *L32(L1_LO + pos[r] * 4) = lo[4 * u + r];
                *L16(L1_C + pos[r] * 2) = (u16)hi[4 * u + r];
            }
        }
        p1_out_store(o0, 4 + (u * 7 + 2) / 5, 4 + ((u + 1) * 7 + 2) / 5, rs0, total0, tid);
        __builtin_amdgcn_sched_barrier(0);
    }
    {
        u32 mx = 0;
#pragma unroll
        for (int u = 0; u < 3; ++u)
#pragma unroll
            for (int k = 0; k < 4; ++k) mx = max(mx, max(mv[u][k] & 0x7fffu, (mv[u][k] >> 16) & 0x7fffu));
        mx = wave_scan_max(mx);
        if ((tid & 63) == 63) __hip_atomic_fetch_max(L32(L1_W + 64), mx, __ATOMIC_RELAXED, __HIP_MEMORY_SCOPE_WORKGROUP);
        if (nanacc != nanacc) *L32(L1_W + 68) = 1u;
    }
    __syncthreads();
    {
        Out o1;
        p1_out_read(o1, tid);
        const auto rs1 = __builtin_amdgcn_make_buffer_rsrc((void*)(rec + (size_t)c1 * (CHP2 * 12)), 0, (u32)CHP2 * 12u, 0x00020000);
        p1_out_store(o1, 0, NOUT, rs1, total1, tid);
    }
    if (tid == 0) meta[blockIdx.x] = *L32(L1_W + 64) | (*L32(L1_W + 68) << 16);
    if (blockIdx.x == 1 && tid < NNULL) {
        const u32 row = RB + ((tid >> 2) & 15);
        u32* p = (u32*)(rec + ((size_t)NULLP + tid) * 12);
        p[0] = row << 16; p[1] = 0u; p[2] = row;
    }
}


struct EpiW { s16x4 wd, wu, ws, wr; u32 cen; };

__device__ __forceinline__ u32 pack_bf(u32 a, u32 b) { return a | (b << 16); }

__device__ __forceinline__ void epi_weights(EpiW& w, const u16* __restrict__ w_rbf, const u16* __restrict__ w_sbf,
                                            const u16* __restrict__ w_down, const u16* __restrict__ w_up,
                                            const u16* __restrict__ centers, int lane) {
    const int fo = lane & 15, g = lane >> 4;
    w.wd = __builtin_bit_cast(s16x4, (u32x2){pack_bf(w_down[(4 * g + 0) * 16 + fo], w_down[(4 * g + 1) * 16 + fo]),
                                             pack_bf(w_down[(4 * g + 2) * 16 + fo], w_down[(4 * g + 3) * 16 + fo])});
    w.wu = __builtin_bit_cast(s16x4, (u32x2){pack_bf(w_up[(4 * g + 0) * 16 + fo], w_up[(4 * g + 1) * 16 + fo]),
                                             pack_bf(w_up[(4 * g + 2) * 16 + fo], w_up[(4 * g + 3) * 16 + fo])});
    const u32 s01 = pack_bf(w_sbf[(2 * g) * 16 + fo], w_sbf[(2 * g + 1) * 16 + fo]);
    const u32 r01 = pack_bf(w_rbf[(2 * g) * 16 + fo], w_rbf[(2 * g + 1) * 16 + fo]);
    w.ws = __builtin_bit_cast(s16x4, (u32x2){s01, s01});
    w.wr = __builtin_bit_cast(s16x4, (u32x2){r01, r01});
    w.cen = pack_bf(centers[2 * g], centers[2 * g + 1]);
}

__device__ __forceinline__ void epi_gates(const EpiW& w, u32 dbits, f32x4& sg, f32x4& gt) {
    const f32x4 z = {0.f, 0.f, 0.f, 0.f};
    const float KE = -14.426950408889634f;
    const float d = bf2f(dbits);
    const float x0 = d - __uint_as_float(w.cen << 16), x1 = d - __uint_as_float(w.cen & 0xffff0000u);
    const float r0 = __builtin_amdgcn_exp2f(x0 * KE * x0), r1 = __builtin_amdgcn_exp2f(x1 * KE * x1);
    const u32 rh = cvt_pk_bf16(r0, r1);
    const u32 rl = cvt_pk_bf16(r0 - __uint_as_float(rh << 16), r1 - __uint_as_float(rh & 0xffff0000u));
    const s16x4 rb = __builtin_bit_cast(s16x4, (u32x2){rh, rl});
    sg = __builtin_amdgcn_mfma_f32_16x16x16bf16_1k(w.ws, rb, z, 0, 0, 0);
    gt = __builtin_amdgcn_mfma_f32_16x16x16bf16_1k(w.wr, rb, z, 0, 0, 0);
}
__device__ __forceinline__ u32x2 epi_rows(const EpiW& w, float a0, float a1, float a2, float a3, float scale,
                                          const f32x4& sg, const f32x4& gt, u32x2 mv) {
    const f32x4 z = {0.f, 0.f, 0.f, 0.f};
    const s16x4 av = __builtin_bit_cast(s16x4, (u32x2){cvt_pk_bf16(a0, a1), cvt_pk_bf16(a2, a3)});
    const f32x4 md = __builtin_amdgcn_mfma_f32_16x16x16bf16_1k(w.wd, av, z, 0, 0, 0);
    const f32x4 ss = sg * scale;
    const s16x4 ag = __builtin_bit_cast(s16x4, (u32x2){cvt_pk_bf16(md[0] * ss[0], md[1] * ss[1]),
                                                       cvt_pk_bf16(md[2] * ss[2], md[3] * ss[3])});
    const f32x4 u = __builtin_amdgcn_mfma_f32_16x16x16bf16_1k(w.wu, ag, z, 0, 0, 0);
    const float o0 = __builtin_fmaf(u[0], gt[0], __uint_as_float(mv[0] << 16));
    const float o1 = __builtin_fmaf(u[1], gt[1], __uint_as_float(mv[0] & 0xffff0000u));
    const float o2 = __builtin_fmaf(u[2], gt[2], __uint_as_float(mv[1] << 16));
    const float o3 = __builtin_fmaf(u[3], gt[3], __uint_as_float(mv[1] & 0xffff0000u));
    return (u32x2){cvt_pk_bf16(o0, o1), cvt_pk_bf16(o2, o3)};
}

__device__ __forceinline__ void decode_pair(u32 alo, u32 ahi, u32 ckl, u32 ckh, float& f0, float& f1) {
    u32 borrow;
    const u32 slo = __builtin_subc(alo, ckl, 0u, &borrow);
    const u32 shi = ahi - ckh - borrow - ckl + (slo >> 31);
    const float fs = (float)(int)slo, fd = (float)(int)shi;
    f0 = fs + fd;
    f1 = fs - fd;
}


struct FE {
    u32 val, cumv;
    u32 n[S];
    u32 rounds[S];
    u32 s, r;
    u32 k00, cap;
    u32 carry, mark, tabl;
    u32 cbase;
};
struct RegSet { u32x3 d[NSB]; u32 nw, soff; bool live; };

__device__ __forceinline__ void fe_fetch(FE& f, RegSet& R, int lane, __amdgpu_buffer_rsrc_t rsr) {
    R.live = f.s < (u32)S;
    const u32 n0 = f.n[0];
    const u32 k00 = f.k00;
    R.nw = R.live ? min(n0 - min(n0, k00), f.cap) : 0u;
    R.soff = min(f.s, (u32)S - 1) * SBYTES;
    if (R.nw == 0) {
        R.d[0] = (u32x3){(u32)(RB + (lane >> 2)) << 16, 0u, (u32)(RB + (lane >> 2))};
        return;
    }
    if (f.r == 0) {
        const u32 t = *(volatile lds_u32*)(size_t)(f.tabl + f.s * 256);
        const u32 len = t >> 16, start = t & 0xffffu;
        const u32 cum = wave_scan_add(len) - len;
        f.val = (lane == 32 ? NULLP - n0 : f.cbase + start - cum) + (f.s << 24);
        f.cumv = lane == 32 ? n0 : (len ? cum : 0x7fffffffu);
    }
    const u32 slot = min(f.cumv - k00, (u32)CAPW + lane);
    *(volatile lds_u32*)(size_t)(f.mark + slot * 4) = f.val;
    u32 v[NSB];
#pragma unroll
    for (int j = 0; j < NSB; ++j) v[j] = *(volatile lds_u32*)(size_t)(f.mark + (j * 64 + lane) * 4);
#pragma unroll
    for (int j = 0; j < NSB; ++j) v[j] = wave_scan_max(v[j]);
    u32 run = f.carry;
#pragma unroll
    for (int j = 0; j < NSB; ++j) {
        v[j] = max(v[j], run);
        run = __builtin_amdgcn_readlane(v[j], 63);
        if ((u32)(j * 64 + 64) <= f.cap) f.carry = run;
    }
#pragma unroll
    for (int j = 0; j < NSB; ++j) {
        if (j == 0 || (u32)(j * 64) < R.nw) {
            const u32 idx = (v[j] & 0xffffffu) + k00 + j * 64 + lane;
            R.d[j] = __builtin_amdgcn_raw_buffer_load_b96(rsr, __umul24(idx, 12u), 0, 0);
        }
    }
}
__device__ __forceinline__ void fe_advance(FE& f) {
    if (f.s >= (u32)S) return;
    f.r += 1;
    f.k00 += f.cap;
    f.cap = CAPW;
    if (f.r >= f.rounds[0]) {
#pragma unroll
        for (int s = 0; s + 1 < S; ++s) { f.n[s] = f.n[s + 1]; f.rounds[s] = f.rounds[s + 1]; }
        f.r = 0;
        f.k00 = 0;
        f.s += 1;
    }
}

struct Gat { u32x2 x0, x1, x2, x3; u32 la, b2; };
struct P2C { u32 q8, q16, psc2; __amdgpu_buffer_rsrc_t rsm; };

__device__ __forceinline__ Gat gat_issue(u32 lo, u32 b2, u32 soff, const P2C& k) {
    Gat g;
    const u32 ga = (lo & 0xffffu) << 5, row = lo >> 16;
    g.x0 = __builtin_amdgcn_raw_buffer_load_b64(k.rsm, quad_bcast<0>(ga) + k.q8, soff, 0);
    g.x1 = __builtin_amdgcn_raw_buffer_load_b64(k.rsm, quad_bcast<1>(ga) + k.q8, soff, 0);
    g.x2 = __builtin_amdgcn_raw_buffer_load_b64(k.rsm, quad_bcast<2>(ga) + k.q8, soff, 0);
    g.x3 = __builtin_amdgcn_raw_buffer_load_b64(k.rsm, quad_bcast<3>(ga) + k.q8, soff, 0);
    g.la = (row << 6) | ((row << 2) & 8u);
    g.b2 = b2;
    lds_add32(L2_CNT + row * 4, 1u);
    asm volatile("" : "+v"(g.la), "+v"(g.b2));
    __builtin_amdgcn_sched_barrier(0);
    return g;
}

__device__ __forceinline__ void gat_consume(const Gat& g, u32 q16) {
    auto step = [&](u32x2 x, u32 laj, u32 bj) {
        float s0, d0, s1, d1;
        asm("v_dot2_f32_bf16 %0, %1, %2, %3" : "=v"(s0) : "v"(x[0]), "v"(bj), "s"(KMAGIC));
        asm("v_dot2_f32_bf16 %0, %1, %2, %3 neg_hi:[0,1,0]" : "=v"(d0) : "v"(x[0]), "v"(bj), "s"(KMAGIC));
        asm("v_dot2_f32_bf16 %0, %1, %2, %3" : "=v"(s1) : "v"(x[1]), "v"(bj), "s"(KMAGIC));
        asm("v_dot2_f32_bf16 %0, %1, %2, %3 neg_hi:[0,1,0]" : "=v"(d1) : "v"(x[1]), "v"(bj), "s"(KMAGIC));
        const u32 a0 = laj + q16;
        lds_add64(a0, ((u64)__float_as_uint(d0) << 32) | __float_as_uint(s0));
        lds_add64(a0 ^ 8u, ((u64)__float_as_uint(d1) << 32) | __float_as_uint(s1));
    };
    step(g.x0, quad_bcast<0>(g.la), quad_bcast<0>(g.b2));
    step(g.x1, quad_bcast<1>(g.la), quad_bcast<1>(g.b2));
    step(g.x2, quad_bcast<2>(g.la), quad_bcast<2>(g.b2));
    step(g.x3, quad_bcast<3>(g.la), quad_bcast<3>(g.b2));
    __builtin_amdgcn_sched_barrier(0);
}

struct Half { u32 loa, lob, b2a, b2b; };
template <int J>
__device__ __forceinline__ Half sb_decode(const RegSet& R, const P2C& k) {
    u32x3 d = R.d[J];
    asm volatile("" : "+v"(d));
    const u32 cp = (d[1] & 0xffffu) | (d[2] & 0xffff0000u);
    const u32 cs = __builtin_bit_cast(u32, __builtin_bit_cast(u16x2, cp) + __builtin_bit_cast(u16x2, k.psc2));
    Half h;
    h.loa = d[0];
    h.lob = __builtin_amdgcn_alignbit(d[2], d[1], 16);
    h.b2a = __builtin_amdgcn_perm(cs, cs, 0x01000100u);
    h.b2b = __builtin_amdgcn_perm(cs, cs, 0x03020302u);
    return h;
}

template <int J>
__device__ __forceinline__ void sb_step(const RegSet& R, u32 nsb, const Half& h, const Gat& g0, const P2C& k) {
    const Gat g1 = gat_issue(h.lob, h.b2b, R.soff, k);
    gat_consume(g0, k.q16);
    if constexpr (J + 1 < NSB) {
        if (nsb > (u32)(J + 1)) {
            const Half hn = sb_decode<J + 1>(R, k);
            if (R.nw <= (u32)((J + 1) * 64 + 32)) {
                const auto ml = __builtin_amdgcn_permlane32_swap(hn.loa, hn.lob, false, false);
                const auto mb = __builtin_amdgcn_permlane32_swap(hn.b2a, hn.b2b, false, false);
                const Gat gn = gat_issue(ml[0], mb[0], R.soff, k);
                gat_consume(g1, k.q16);
                gat_consume(gn, k.q16);
                return;
            }
            const Gat gn = gat_issue(hn.loa, hn.b2a, R.soff, k);
            gat_consume(g1, k.q16);
            sb_step<J + 1>(R, nsb, hn, gn, k);
            return;
        }
    }
    gat_consume(g1, k.q16);
}

__global__ __launch_bounds__(1024) void p2_accumulate(const char* __restrict__ rec, const u32* __restrict__ tab,
                                                      const u32* __restrict__ meta, const u16* __restrict__ m,
                                                      const u16* __restrict__ dist, const u16* __restrict__ centers,
                                                      const u16* __restrict__ w_rbf, const u16* __restrict__ w_sbf,
                                                      const u16* __restrict__ w_down, const u16* __restrict__ w_up,
                                                      u16* __restrict__ out) {
    const int tid = threadIdx.x, lane = tid & 63;
    const int wave = __builtin_amdgcn_readfirstlane(tid >> 6);
    const int b = (blockIdx.x & 7) * 32 + (blockIdx.x >> 3);
    const int row0 = b * RB;
    const auto rsr = __builtin_amdgcn_make_buffer_rsrc((void*)rec, 0, (u32)(WS_TAB), 0x00020000);
    const auto rsm = __builtin_amdgcn_make_buffer_rsrc((void*)m, 0, (u32)E_ * 32u, 0x00020000);
    const auto rst = __builtin_amdgcn_make_buffer_rsrc((void*)tab, 0, (u32)NC * NB * S * 4u, 0x00020000);
    const auto rsq = __builtin_amdgcn_make_buffer_rsrc((void*)meta, 0, 1024u, 0x00020000);
    const int c = (lane & 31) * 16 + wave;
    const u32 toff = lane < 32 ? (u32)(c * NB + b) * (S * 4) : 0x80000000u;
    u32 t[S];
    {
        const u32x4 t0 = __builtin_amdgcn_raw_buffer_load_b128(rst, toff, 0, 0);
        t[0] = t0[0]; t[1] = t0[1]; t[2] = t0[2]; t[3] = t0[3];
        t[4] = __builtin_amdgcn_raw_buffer_load_b32(rst, toff + 16, 0, 0);
    }
    const u32x4 mt4 = __builtin_amdgcn_raw_buffer_load_b128(rsq, lane * 16, 0, 0);
    __builtin_amdgcn_sched_barrier(0);
    const u32 mark = L2_MARK + wave * ((CAPW + 64) * 4);
    *(lds_u32x4*)(size_t)(mark + lane * 16) = (u32x4){0u, 0u, 0u, 0u};
    if (lane < 32) *(lds_u32x4*)(size_t)(mark + 1024 + lane * 16) = (u32x4){0u, 0u, 0u, 0u};
    static_assert((CAPW + 64) * 4 == 1536, "marker area");
    for (u32 a = tid * 16; a < L2_MARK; a += 1024 * 16) *(lds_u32x4*)(size_t)a = (u32x4){0u, 0u, 0u, 0u};
    if (tid == 0) *L32(L2_FLAG) = 0u;
    const u32 mtm = max(max(mt4[0] & 0xffffu, mt4[1] & 0xffffu), max(mt4[2] & 0xffffu, mt4[3] & 0xffffu));
    const u32 mmax = __builtin_amdgcn_readlane(wave_scan_max(mtm), 63);
    const bool anynan = __builtin_amdgcn_ballot_w64(((mt4[0] | mt4[1] | mt4[2] | mt4[3]) & 0x10000u) != 0) != 0;
    const u32 eb = mmax >> 7;
    const bool fast = !anynan && (mmax == 0 || (eb >= 64 && eb <= 147));
    const u32 ebc = mmax == 0 ? 127u : min(max(eb, 64u), 147u);
    const u32 pexp = 147u - ebc;
    const float inv2 = __uint_as_float((126u - pexp) << 23);
    const int e = lane & 15, g = lane >> 4;
    EpiW w;
    bool slow = !fast;
    if (fast) {
        FE f;
        f.tabl = L2_TABW + wave * (S * 256) + lane * 4;
        f.cbase = (u32)c * CHP2;
#pragma unroll
        for (int s = 0; s < S; ++s) {
            f.n[s] = __builtin_amdgcn_readlane(wave_scan_add(t[s] >> 16), 63);
            *L32(f.tabl + s * 256) = t[s];
        }
        f.s = 0; f.r = 0; f.carry = 0; f.mark = mark;
        f.k00 = 0; f.cap = CAP0;
        P2C k;
        k.q8 = (lane & 3) * 8; k.q16 = (lane & 3) * 16 + L2_ACC;
        k.psc2 = (pexp << 7) * 0x10001u; k.rsm = rsm;
        RegSet cur, nxt;
        fe_fetch(f, nxt, lane, rsr);
        if (lane < S) {
            u32 v = f.n[0];
#pragma unroll
            for (int s = 1; s < S; ++s) v = lane == s ? f.n[s] : v;
            *L32(L2_NW + lane * 64 + wave * 4) = v;
        }
        cur = nxt;
#pragma unroll
        for (int j = 0; j < NSB; ++j) asm volatile("" : "+v"(cur.d[j]));
        __syncthreads();
#pragma unroll
        for (int s = 0; s < S; ++s) {
            const u32 v = *L32(L2_NW + s * 64 + (lane & 15) * 4);
            const u32 mx = __builtin_amdgcn_readlane(wave_scan_max(v), 63);
            f.rounds[s] = s == 0 ? 1u + (mx - min(mx, (u32)CAP0) + CAPW - 1) / CAPW : max((mx + CAPW - 1) / CAPW, 1u);
        }
        fe_advance(f);
        Half h = sb_decode<0>(cur, k);
        Gat g0 = gat_issue(h.loa, h.b2a, cur.soff, k);
        fe_fetch(f, nxt, lane, rsr);
        fe_advance(f);
        if (wave == 0) {
            epi_weights(w, w_rbf, w_sbf, w_down, w_up, centers, lane);
            *(lds_u32x2*)(size_t)(L2_WD + lane * 8) = __builtin_bit_cast(u32x2, w.wd);
            *(lds_u32x2*)(size_t)(L2_WU + lane * 8) = __builtin_bit_cast(u32x2, w.wu);
            *(lds_u32x2*)(size_t)(L2_WS + lane * 8) = __builtin_bit_cast(u32x2, w.ws);
            *(lds_u32x2*)(size_t)(L2_WR + lane * 8) = __builtin_bit_cast(u32x2, w.wr);
            *L32(L2_CEN + lane * 4) = w.cen;
        } else if (wave < 4) {
#pragma unroll
            for (int u = 0; u < 4; ++u) {
                const int r = ((wave - 1) * 4 + u) * 128 + lane * 2;
                if (r < RB) *L32(L2_DIST + r * 2) = *(const u32*)(dist + row0 + r);
            }
        }
#pragma clang loop unroll(disable)
        while (cur.live) {
            sb_step<0>(cur, (cur.nw + 63) >> 6, h, g0, k);
            cur = nxt;
#pragma unroll
            for (int j = 0; j < NSB; ++j) asm volatile("" : "+v"(cur.d[j]));
            h = sb_decode<0>(cur, k);
            g0 = gat_issue(h.loa, h.b2a, cur.soff, k);
            fe_fetch(f, nxt, lane, rsr);
            fe_advance(f);
            __syncthreads();
        }
        asm volatile("" :: "v"(g0.x0), "v"(g0.x1), "v"(g0.x2), "v"(g0.x3));
        const auto rso = __builtin_amdgcn_make_buffer_rsrc((void*)(out + (size_t)row0 * 16), 0, (u32)RB * 32u, 0x00020000);
        const auto rsi = __builtin_amdgcn_make_buffer_rsrc((void*)(m + (size_t)row0 * 16), 0, (u32)RB * 32u, 0x00020000);
        u32x2 mv[5];
        f32x4 sg[5], gt[5];
#pragma unroll
        for (int u = 0; u < 5; ++u) mv[u] = __builtin_amdgcn_raw_buffer_load_b64(rsi, ((wave + 16 * u) * 16 + e) * 32 + g * 8, 0, 0);
        w.wd = __builtin_bit_cast(s16x4, *(lds_u32x2*)(size_t)(L2_WD + lane * 8));
        w.wu = __builtin_bit_cast(s16x4, *(lds_u32x2*)(size_t)(L2_WU + lane * 8));
        w.ws = __builtin_bit_cast(s16x4, *(lds_u32x2*)(size_t)(L2_WS + lane * 8));
        w.wr = __builtin_bit_cast(s16x4, *(lds_u32x2*)(size_t)(L2_WR + lane * 8));
        w.cen = *L32(L2_CEN + lane * 4);
#pragma unroll
        for (int u = 0; u < 5; ++u) {
            const int rc = min((wave + 16 * u) * 16 + e, RB - 1);
            epi_gates(w, *L16(L2_DIST + rc * 2), sg[u], gt[u]);
        }
        u32 over = 0;
#pragma unroll
        for (int u = 0; u < 5; ++u) {
            const int r = (wave + 16 * u) * 16 + e;
            const int rc = min(r, RB - 1);
            const u32 aa = L2_ACC + rc * 64 + g * 16 + ((rc << 2) & 8);
            const u32x2 p0 = *(lds_u32x2*)(size_t)aa, p1 = *(lds_u32x2*)(size_t)(aa ^ 8);
            const u32 cn = *L32(L2_CNT + rc * 4);
            over = max(over, cn);
            const u32 q = __umul24(cn, KBITS >> 22);
            const u32 ckl = q << 22, ckh = q >> 10;
            float a0, a1, a2, a3;
            decode_pair(p0[0], p0[1], ckl, ckh, a0, a1);
            decode_pair(p1[0], p1[1], ckl, ckh, a2, a3);
            const u32x2 o = epi_rows(w, a0, a1, a2, a3, inv2, sg[u], gt[u], mv[u]);
            __builtin_amdgcn_raw_buffer_store_b64(o, rso, r * 32 + g * 8, 0, AUX_WT);
        }
        if (over > 511u) *L32(L2_FLAG) = 1u;
        asm volatile("s_waitcnt vmcnt(0)" ::: "memory");
        __syncthreads();
        slow = *L32(L2_FLAG) != 0;
    }
    if (!slow) return;
    const u32x2* m2 = (const u32x2*)m;
    u32x2* out2 = (u32x2*)out;
    epi_weights(w, w_rbf, w_sbf, w_down, w_up, centers, lane);
    for (int half = 0; half < 2; ++half) {
        __syncthreads();
        for (u32 a = tid * 16; a < 625 * 128; a += 1024 * 16) *(lds_u32x4*)(size_t)a = (u32x4){0u, 0u, 0u, 0u};
        __syncthreads();
        for (int s = 0; s < S; ++s) {
            for (int i = 0; i < 32; ++i) {
                const int cc = i * 16 + wave;
                const u32 tt = tab[((size_t)cc * NB + b) * S + s];
                const u32 len = (tt >> 16) * 2, start = (tt & 0xffffu) * 2;
                for (u32 j0 = 0; j0 < len; j0 += 4) {
                    const u32 j = j0 + (lane >> 4);
                    if (j < len) {
                        const u16* rp = (const u16*)(rec + ((size_t)cc * CHP2 * 2 + start + j) * 6);
                        const u32 il = rp[0], ol = rp[1], cb = rp[2];
                        const int r = (int)ol - half * 625;
                        if (r >= 0 && r < 625) {
                            const float x = bf2f(m[((size_t)s * SR + il) * 16 + (lane & 15)]);
                            __hip_atomic_fetch_add((lds_f64*)(size_t)(r * 128 + (lane & 15) * 8), (double)(bf2f(cb) * x),
                                                   __ATOMIC_RELAXED, __HIP_MEMORY_SCOPE_WORKGROUP);
                        }
                    }
                }
            }
        }
        __syncthreads();
        for (int gi = wave; gi < 40; gi += 16) {
            const int r = gi * 16 + e;
            const bool valid = r < 625;
            const int rc = valid ? r : 624;
            const int grow = row0 + half * 625 + rc;
            const lds_f64* p = (const lds_f64*)(size_t)(rc * 128 + g * 32);
            f32x4 sg, gt;
            epi_gates(w, dist[grow], sg, gt);
            const u32x2 o = epi_rows(w, (float)p[0], (float)p[1], (float)p[2], (float)p[3], 1.0f, sg, gt, m2[(size_t)grow * 4 + g]);
            if (valid) out2[(size_t)grow * 4 + g] = o;
        }
    }
}

}

extern "C" void kernel_launch(void* const* d_in, const int* in_sizes, int n_in, void* d_out, int out_size, void* d_ws,
                              size_t ws_size, hipStream_t stream) {
    const u16* m = (const u16*)d_in[0];
    const int* idx_in = (const int*)d_in[1];
    const int* idx_out = (const int*)d_in[2];
    const u16* dist = (const u16*)d_in[3];
    const u16* angle = (const u16*)d_in[4];
    const u16* centers = (const u16*)d_in[5];
    const u16* ang_freq = (const u16*)d_in[6];
    const u16* w_rbf = (const u16*)d_in[7];
    const u16* w_sbf = (const u16*)d_in[8];
    const u16* w_down = (const u16*)d_in[9];
    const u16* w_up = (const u16*)d_in[10];
    char* ws = (char*)d_ws;
    (void)hipFuncSetAttribute((const void*)p1_partition, hipFuncAttributeMaxDynamicSharedMemorySize, L1_SIZE);
    (void)hipFuncSetAttribute((const void*)p2_accumulate, hipFuncAttributeMaxDynamicSharedMemorySize, L2_SIZE);
    p1_partition<<<NWG1, 1024, L1_SIZE, stream>>>((const u32x4*)idx_in, (const u32x4*)idx_out, (const u32x2*)angle, ang_freq,
                                                  (const u32x4*)m, ws + WS_REC, (u32*)(ws + WS_TAB), (u32*)(ws + WS_META));
    p2_accumulate<<<NB, 1024, L2_SIZE, stream>>>(ws + WS_REC, (const u32*)(ws + WS_TAB), (const u32*)(ws + WS_META), m, dist,
                                                 centers, w_rbf, w_sbf, w_down, w_up, (u16*)d_out);
}
```
